```python
import torch

def _softmax(x, axis=-1):
    return torch.softmax(x, dim=axis)

def _gelu(x):
    return torch.nn.functional.gelu(x, approximate='tanh')

def _silu(x):
    return torch.nn.functional.silu(x)

def _sigmoid(x):
    return torch.sigmoid(x)

def _layernorm(x, axis=-1, eps=1e-05):
    m = x.mean(dim=axis, keepdim=True)
    v = x.var(dim=axis, keepdim=True, unbiased=False)
    return (x - m) / torch.sqrt(v + eps)

def _scatter_add(tgt, idx, src, axis=0):
    return tgt.clone().index_add_(axis, idx, src)

def _segment_sum(src, seg, n):
    out = torch.zeros((n,) + src.shape[1:], dtype=src.dtype)
    return out.index_add_(0, seg, src)

def _pad(x, pw):
    flat = tuple((v for pair in reversed(pw) for v in pair))
    return torch.nn.functional.pad(x, flat)
kernel_name = 'adaLN_cond__spectral_fnet__dense_activation_relu__prompt'

def setup_inputs():

    _w = lambda a: (lambda t: t.to(torch.bfloat16) if t.is_floating_point() else t)(torch.as_tensor(a))

    def _A_setup():
        B, S, D = (1, 2048, 512)
        ns = torch.arange(S)[:, None] * torch.arange(S)[None, :]
        nd = torch.arange(D)[:, None] * torch.arange(D)[None, :]
        Ws_re = torch.cos(-2.0 * torch.pi * ns / S).to(float)
        Ws_im = torch.sin(-2.0 * torch.pi * ns / S).to(float)
        Wd_re = torch.cos(-2.0 * torch.pi * nd / D).to(float)
        Wd_im = torch.sin(-2.0 * torch.pi * nd / D).to(float)
        return {'x': _w((lambda *s: torch.randn(*s, dtype=torch.bfloat16) * 0.1)(B, S, D)), 'dft_seq_re': _w(Ws_re), 'dft_seq_im': _w(Ws_im), 'dft_hid_re': _w(Wd_re), 'dft_hid_im': _w(Wd_im)}
    _A_in = _A_setup()

    def _B_setup():
        B, S, D, Dff = (1, 2048, 512, 1792)
        return {'x': _w((lambda *s: torch.randn(*s, dtype=torch.bfloat16) * 0.1)(B, S, D)), 'w1': _w((lambda *s: torch.randn(*s, dtype=torch.bfloat16) * 3.2)(D, Dff)), 'w2': _w((lambda *s: torch.randn(*s, dtype=torch.bfloat16) * 3.2)(Dff, D))}
    _B_in = _B_setup()
    return {'A_x': _A_in['x'], 'A_dft_seq_re': _A_in['dft_seq_re'], 'A_dft_seq_im': _A_in['dft_seq_im'], 'A_dft_hid_re': _A_in['dft_hid_re'], 'A_dft_hid_im': _A_in['dft_hid_im'], 'B_x': _B_in['x'], 'B_w1': _B_in['w1'], 'B_w2': _B_in['w2'], 'c': _w((lambda *s: torch.randn(*s, dtype=torch.bfloat16) * 0.4)(1, 512) * 8.0), 'w_mod': _w((lambda *s: torch.randn(*s, dtype=torch.bfloat16) * 0.4)(512, 6 * 512) * 512 ** (-0.5) * 8.0)}

def reference(A_x, A_dft_seq_re, A_dft_seq_im, A_dft_hid_re, A_dft_hid_im, B_x, B_w1, B_w2, c, w_mod):

    def _A_ref(x, dft_seq_re, dft_seq_im, dft_hid_re, dft_hid_im):
        a = torch.einsum('bsd,ed->bse', x, dft_hid_re)
        b = torch.einsum('bsd,ed->bse', x, dft_hid_im)
        y = torch.einsum('ts,bse->bte', dft_seq_re, a) - torch.einsum('ts,bse->bte', dft_seq_im, b)
        return y

    def _B_ref(x, w1, w2):
        h = torch.einsum('bsd,df->bsf', x, w1)
        zero = torch.zeros_like(h)
        h = torch.maximum(h, zero)
        return torch.einsum('bsf,fd->bsd', h, w2)
    x = A_x
    mod = (_silu(c) @ w_mod).reshape(c.shape[0], 6, 1, -1)
    sa, ca, ga, sb, cb, gb = (mod[:, 0].clone(), mod[:, 1].clone(), mod[:, 2].clone(), mod[:, 3].clone(), mod[:, 4].clone(), mod[:, 5].clone())
    h = _layernorm(x, axis=-1) * (1 + ca) + sa
    x = x + ga * _A_ref(h, A_dft_seq_re, A_dft_seq_im, A_dft_hid_re, A_dft_hid_im)
    h = _layernorm(x, axis=-1) * (1 + cb) + sb
    x = x + gb * _B_ref(h, B_w1, B_w2)
    return x
```

```cpp
#include <hip/hip_runtime.h>
#include <hip/hip_bf16.h>
#include <stdint.h>

typedef short bf16x8 __attribute__((ext_vector_type(8)));
typedef float f32x16 __attribute__((ext_vector_type(16)));
typedef unsigned short u16;

constexpr int S = 2048, D = 512, F = 1792;

__device__ __forceinline__ float bf2f(u16 b) { return __builtin_bit_cast(float, (unsigned)b << 16); }
__device__ __forceinline__ u16 f2bf(float f) { __bf16 h = (__bf16)f; return __builtin_bit_cast(u16, h); }

__global__ void k_mod(const u16* c, const u16* wmod, float* mod) {
    int j = blockIdx.x * 64 + threadIdx.x;
    float acc = 0.f;
    for (int k = 0; k < D; ++k) {
        float cv = bf2f(c[k]);
        float s = cv / (1.f + __expf(-cv));
        acc += s * bf2f(wmod[k * 6 * D + j]);
    }
    mod[j] = acc;
}

__global__ void k_rowsum(const u16* wre, const u16* wim, float* rs) {
    int r = blockIdx.x;
    const u16* p = r < S ? wre + (size_t)r * S : wim + (size_t)(r - S) * S;
    float a = 0.f;
    for (int k = threadIdx.x; k < S; k += 64) a += bf2f(p[k]);
    for (int o = 32; o; o >>= 1) a += __shfl_xor(a, o);
    if (threadIdx.x == 0) rs[r] = a;
}

template <int MODE>
__global__ void k_ln(const u16* x, const float* x1, const float* mod, u16* out) {
    int r = blockIdx.x, l = threadIdx.x;
    float v[8];
    float s = 0.f;
    for (int j = 0; j < 8; ++j) { v[j] = MODE == 0 ? bf2f(x[r * D + l * 8 + j]) : x1[r * D + l * 8 + j]; s += v[j]; }
    for (int o = 32; o; o >>= 1) s += __shfl_xor(s, o);
    float m = s / D, q = 0.f;
    for (int j = 0; j < 8; ++j) { v[j] -= m; q += v[j] * v[j]; }
    for (int o = 32; o; o >>= 1) q += __shfl_xor(q, o);
    float rs = rsqrtf(q / D + 1e-5f);
    for (int j = 0; j < 8; ++j) {
        int d = l * 8 + j;
        if (MODE == 0) out[d * S + r] = f2bf(v[j] * rs);
        else out[r * D + d] = f2bf(v[j] * rs * (1.f + mod[4 * D + d]) + mod[3 * D + d]);
    }
}

template <bool BT>
__device__ __forceinline__ f32x16 gemm_tile(const u16* A, int lda, const u16* B, int ldb, int K, int m0, int n0, f32x16 acc) {
    int l = threadIdx.x, r = l & 31, h = l >> 5;
    for (int k = 0; k < K; k += 16) {
        bf16x8 a = *(const bf16x8*)(A + (size_t)(m0 + r) * lda + k + 8 * h);
        bf16x8 b;
        if (BT) b = *(const bf16x8*)(B + (size_t)(n0 + r) * ldb + k + 8 * h);
        else for (int j = 0; j < 8; ++j) b[j] = (short)B[(size_t)(k + 8 * h + j) * ldb + n0 + r];
        acc = __builtin_amdgcn_mfma_f32_32x32x16_bf16(a, b, acc, 0, 0, 0);
    }
    return acc;
}
#define ROW(reg) (((reg) & 3) + 8 * ((reg) >> 2) + 4 * (threadIdx.x >> 5))

__global__ void k_s1(const u16* wre, const u16* wim, const u16* xnT, const float* mod, const float* rs, u16* pq) {
    int m0 = blockIdx.x * 32, n0 = blockIdx.y * 32;
    bool im = m0 >= S;
    f32x16 acc = {};
    acc = gemm_tile<true>(im ? wim : wre, S, xnT, S, S, im ? m0 - S : m0, n0, acc);
    int d = n0 + (threadIdx.x & 31);
    float sc = 1.f + mod[D + d], sh = mod[d];
    for (int i = 0; i < 16; ++i) {
        int row = m0 + ROW(i);
        float v = acc[i] * sc + rs[row] * sh;
        if (im) pq[(size_t)(row - S) * 2 * D + D + d] = f2bf(-v);
        else pq[(size_t)row * 2 * D + d] = f2bf(v);
    }
}
__global__ void k_s2(const u16* pq, const u16* dre, const u16* dim_, const u16* x, const float* mod, float* x1) {
    int m0 = blockIdx.x * 32, n0 = blockIdx.y * 32;
    f32x16 acc = {};
    acc = gemm_tile<true>(pq, 2 * D, dre, D, D, m0, n0, acc);
    acc = gemm_tile<true>(pq + D, 2 * D, dim_, D, D, m0, n0, acc);
    int e = n0 + (threadIdx.x & 31);
    float g = mod[2 * D + e];
    for (int i = 0; i < 16; ++i) {
        int row = m0 + ROW(i);
        x1[row * D + e] = bf2f(x[row * D + e]) + g * acc[i];
    }
}
__global__ void k_s3(const u16* h2, const u16* w1, u16* hh) {
    int m0 = blockIdx.x * 32, n0 = blockIdx.y * 32;
    f32x16 acc = {};
    acc = gemm_tile<false>(h2, D, w1, F, D, m0, n0, acc);
    int f = n0 + (threadIdx.x & 31);
    for (int i = 0; i < 16; ++i) hh[(size_t)(m0 + ROW(i)) * F + f] = f2bf(fmaxf(acc[i], 0.f));
}
__global__ void k_s4(const u16* hh, const u16* w2, const float* x1, const float* mod, u16* out) {
    int m0 = blockIdx.x * 32, n0 = blockIdx.y * 32;
    f32x16 acc = {};
    acc = gemm_tile<false>(hh, F, w2, D, F, m0, n0, acc);
    int e = n0 + (threadIdx.x & 31);
    float g = mod[5 * D + e];
    for (int i = 0; i < 16; ++i) {
        int row = m0 + ROW(i);
        out[row * D + e] = f2bf(x1[row * D + e] + g * acc[i]);
    }
}

extern "C" void kernel_launch(void* const* d_in, const int* in_sizes, int n_in, void* d_out, int out_size,
                              void* d_ws, size_t ws_size, hipStream_t stream) {
    const u16* x = (const u16*)d_in[0];
    const u16* wre = (const u16*)d_in[1];
    const u16* wim = (const u16*)d_in[2];
    const u16* dre = (const u16*)d_in[3];
    const u16* dim_ = (const u16*)d_in[4];
    const u16* w1 = (const u16*)d_in[6];
    const u16* w2 = (const u16*)d_in[7];
    const u16* c = (const u16*)d_in[8];
    const u16* wmod = (const u16*)d_in[9];
    char* ws = (char*)d_ws;
    float* mod = (float*)ws;
    float* rs = (float*)(ws + 16384);
    u16* xnT = (u16*)(ws + (1 << 20));
    u16* pq = (u16*)(ws + (4 << 20));
    float* x1 = (float*)(ws + (8 << 20));
    u16* h2 = (u16*)(ws + (12 << 20));
    u16* hh = (u16*)(ws + (16 << 20));
    k_mod<<<6 * D / 64, 64, 0, stream>>>(c, wmod, mod);
    k_rowsum<<<2 * S, 64, 0, stream>>>(wre, wim, rs);
    k_ln<0><<<S, 64, 0, stream>>>(x, nullptr, mod, xnT);
    k_s1<<<dim3(2 * S / 32, D / 32), 64, 0, stream>>>(wre, wim, xnT, mod, rs, pq);
    k_s2<<<dim3(S / 32, D / 32), 64, 0, stream>>>(pq, dre, dim_, x, mod, x1);
    k_ln<1><<<S, 64, 0, stream>>>(nullptr, x1, mod, h2);
    k_s3<<<dim3(S / 32, F / 32), 64, 0, stream>>>(h2, w1, hh);
    k_s4<<<dim3(S / 32, D / 32), 64, 0, stream>>>(hh, w2, x1, mod, (u16*)d_out);
}
```

```cpp
#include <hip/hip_runtime.h>
#include <hip/hip_bf16.h>

__global__ void __launch_bounds__(256, 1) __attribute__((aligned(4096)))
adaln_fnet_ffn_kernel(const void* x, const void* wre, const void* wim, const void* dre, const void* dim, const void* w1,
                      const void* w2, const void* c, const void* wmod, void* out, void* ws) {
#if defined(__HIP_DEVICE_COMPILE__)
    __shared__ __attribute__((aligned(16))) char smem[163840];
    asm volatile(
        ".Lkbeg:\n"
        "s_load_dwordx16 s[64:79], s[0:1], 0x0\n"
        "s_load_dwordx4 s[80:83], s[0:1], 0x40\n"
        "s_load_dwordx2 s[84:85], s[0:1], 0x50\n"
        "v_readfirstlane_b32 s50, v0\n"
        "v_and_b32 v1, 63, v0\n"
        "s_nop 0\n"
        "s_lshr_b32 s50, s50, 6\n"
        "s_getpc_b64 s[48:49]\n"
        ".Lkpc:\n"
        "s_lshr_b32 s55, s2, 3\n"
        "s_lshl_b32 s55, s55, 2\n"
        "s_add_u32 s55, s55, s50\n"
        "s_lshl_b32 s55, s55, 9\n"
        "v_and_b32 v6, 3, v0\n"
        "v_lshl_add_u32 v6, v6, 7, s55\n"
        "v_min_u32 v6, .Lkend-.Lkpc-4, v6\n"
        "global_load_dword v9, v6, s[48:49]\n"
        "s_bfe_u32 s51, s2, 0x30003\n"
        "s_and_b32 s53, s2, 7\n"
        "s_lshr_b32 s55, s2, 6\n"
        "s_lshl_b32 s52, s53, 2\n"
        "s_add_u32 s52, s52, s55\n"
        "s_mov_b32 s54, 0x40000\n"
        "v_lshlrev_b32 v2, 4, v1\n"
        "v_and_b32 v4, 15, v0\n"
        "v_and_b32 v3, 48, v0\n"
        "v_bfe_u32 v7, v0, 4, 1\n"
        "v_lshlrev_b32 v7, 2, v7\n"
        "v_and_b32 v8, 7, v0\n"
        "v_xor_b32 v7, v7, v8\n"
        "v_lshlrev_b32 v10, 4, v7\n"
        "v_lshrrev_b32 v11, 3, v1\n"
        "v_mul_u32_u24 v12, 6144, v11\n"
        "v_add_u32 v12, v12, v10\n"
        "v_and_b32 v8, 0x1f0, v2\n"
        "v_and_b32 v7, 32, v0\n"
        "v_lshl_add_u32 v13, v7, 11, v8\n"
        "v_lshlrev_b32 v14, 2, v1\n"
        "v_mov_b32 v15, 0\n"
        "s_mov_b32 s7, 0x20000\n"
        "s_mov_b32 s11, 0x20000\n"
        "s_mov_b32 s15, 0x20000\n"
        "s_mov_b32 s23, 0x20000\n"
        "s_mov_b32 s27, 0x20000\n"
        "s_mov_b32 s31, 0x20000\n"
        "s_mov_b32 s35, 0x20000\n"
        "s_mov_b32 s39, 0x20000\n"
        "s_mov_b32 s43, 0x20000\n"
        "s_mov_b32 s47, 0x20000\n"
        "s_mov_b32 s6, 0x200000\n"
        "s_mov_b32 s10, 0x800000\n"
        "s_mov_b32 s14, 0x80000\n"
        "s_mov_b32 s22, 0x1c0000\n"
        "s_mov_b32 s26, 0x1c0000\n"
        "s_mov_b32 s34, 0x400\n"
        "s_mov_b32 s38, 0x200000\n"
        "s_mov_b32 s42, 0x2c00000\n"
        "s_cmp_lt_u32 s51, 6\n"
        "s_cselect_b32 s30, 0x300000, 0\n"
        "s_cselect_b32 s46, 0x2c00000, 0\n"
        "s_lshl_b32 s60, s52, 3\n"
        "s_add_u32 s60, s60, s51\n"
        "s_mul_i32 s55, s52, 0x6\n"
        "s_add_u32 s55, s55, s51\n"
        "s_and_b32 s61, s55, 3\n"
        "s_lshr_b32 s62, s55, 2\n"
        "s_and_b32 s63, s51, 3\n"
        "s_and_b32 s88, s53, 1\n"
        "s_lshr_b32 s90, s53, 1\n"
        "s_lshl_b32 s89, s90, 3\n"
        "s_lshr_b32 s55, s2, 5\n"
        "s_add_u32 s89, s89, s55\n"
        "s_add_u32 s90, s90, s63\n"
        "s_waitcnt lgkmcnt(0)\n"
        "s_mov_b32 s4, s64\n"
        "s_and_b32 s5, s65, 0xffff\n"
        "s_mov_b32 s40, s84\n"
        "s_and_b32 s41, s85, 0xffff\n"
        "s_lshl_b32 s55, s60, 2\n"
        "s_add_u32 s55, s55, s50\n"
        "s_lshl_b32 s55, s55, 11\n"
        "buffer_load_dwordx4 v[244:247], v2, s[4:7], s55 offen\n"
        "buffer_load_dwordx4 v[248:251], v2, s[4:7], s55 offen offset:1024\n"
        "buffer_load_dwordx2 v[240:241], v15, s[40:43], 0 offen sc1\n"
        "s_mov_b32 s32, s78\n"
        "s_and_b32 s33, s79, 0xffff\n"
        "s_mov_b32 s28, s80\n"
        "s_and_b32 s29, s81, 0xffff\n"
        "s_lshl_b32 s55, s61, 8\n"
        "buffer_load_dword v16, v14, s[32:35], s55 offen\n"
        "s_lshl_b32 s55, s61, 2\n"
        "s_add_u32 s55, s55, s50\n"
        "s_mul_i32 s55, s55, 0x30000\n"
        "s_lshl_b32 s58, s62, 7\n"
        "s_add_u32 s55, s55, s58\n"
        "s_lshl_b32 s58, s50, 12\n"
        "s_add_u32 m0, s58, 0x21000\n"
        "s_nop 0\n"
        "buffer_load_dwordx4 v12, s[28:31], s55 offen lds\n"
        "s_add_u32 m0, s58, 0x21400\n"
        "s_add_u32 s55, s55, 0xc000\n"
        "buffer_load_dwordx4 v12, s[28:31], s55 offen lds\n"
        "s_add_u32 m0, s58, 0x21800\n"
        "s_add_u32 s55, s55, 0xc000\n"
        "buffer_load_dwordx4 v12, s[28:31], s55 offen lds\n"
        "s_add_u32 m0, s58, 0x21c00\n"
        "s_add_u32 s55, s55, 0xc000\n"
        "buffer_load_dwordx4 v12, s[28:31], s55 offen lds\n"
        "s_cmp_lt_u32 s50, 2\n"
        "s_cselect_b64 s[8:9], s[66:67], s[68:69]\n"
        "s_and_b32 s9, s9, 0xffff\n"
        "s_mul_i32 s91, s50, 0x2100\n"
        "s_and_b32 s55, s50, 1\n"
        "s_lshl_b32 s58, s89, 3\n"
        "s_add_u32 s55, s55, s58\n"
        "s_lshl_b32 s92, s55, 15\n"
        "s_mul_i32 s55, s88, 0x20800\n"
        "s_add_u32 s92, s92, s55\n"
        "s_add_u32 s93, s90, 0x0\n"
        "s_and_b32 s93, s93, 3\n"
        "s_lshl_b32 s93, s93, 9\n"
        "s_add_u32 s93, s93, s92\n"
        "s_add_u32 m0, s91, 0x0\n"
        "s_xor_b32 s93, s93, 0x20000\n"
        "buffer_load_dwordx4 v13, s[8:11], s93 offen lds\n"
        "s_add_u32 m0, s91, 0x420\n"
        "s_addk_i32 s93, 4096\n"
        "buffer_load_dwordx4 v13, s[8:11], s93 offen lds\n"
        "s_mov_b32 s20, s74\n"
        "s_and_b32 s21, s75, 0xffff\n"
        "s_mov_b32 s24, s76\n"
        "s_and_b32 s25, s77, 0xffff\n"
        "s_mov_b32 s36, s82\n"
        "s_and_b32 s37, s83, 0xffff\n"
        "s_mov_b32 s44, s84\n"
        "s_and_b32 s45, s85, 0xffff\n"
        "s_lshr_b32 s95, s76, 7\n"
        "s_sub_u32 s95, s51, s95\n"
        "s_add_u32 s95, s95, 0x4\n"
        "s_and_b32 s95, s95, 7\n"
        "s_mov_b64 s[16:17], s[70:71]\n"
        "s_mov_b64 s[18:19], s[72:73]\n"
        "s_waitcnt vmcnt(8)\n"
        "v_lshlrev_b32 v224, 16, v244\n"
        "v_and_b32 v225, 0xffff0000, v244\n"
        "v_lshlrev_b32 v226, 16, v245\n"
        "v_and_b32 v227, 0xffff0000, v245\n"
        "v_lshlrev_b32 v228, 16, v246\n"
        "v_and_b32 v229, 0xffff0000, v246\n"
        "v_lshlrev_b32 v230, 16, v247\n"
        "v_and_b32 v231, 0xffff0000, v247\n"
        "v_lshlrev_b32 v232, 16, v248\n"
        "v_and_b32 v233, 0xffff0000, v248\n"
        "v_lshlrev_b32 v234, 16, v249\n"
        "v_and_b32 v235, 0xffff0000, v249\n"
        "v_lshlrev_b32 v236, 16, v250\n"
        "v_and_b32 v237, 0xffff0000, v250\n"
        "v_lshlrev_b32 v238, 16, v251\n"
        "v_and_b32 v239, 0xffff0000, v251\n"
        "v_pk_add_f32 v[216:217], v[224:225], v[226:227]\n"
        "v_pk_mul_f32 v[218:219], v[224:225], v[224:225]\n"
        "v_pk_add_f32 v[216:217], v[216:217], v[228:229]\n"
        "v_pk_fma_f32 v[218:219], v[226:227], v[226:227], v[218:219]\n"
        "v_pk_add_f32 v[216:217], v[216:217], v[230:231]\n"
        "v_pk_fma_f32 v[218:219], v[228:229], v[228:229], v[218:219]\n"
        "v_add_f32 v216, v216, v217\n"
        "v_pk_fma_f32 v[218:219], v[230:231], v[230:231], v[218:219]\n"
        "v_add_f32 v218, v218, v219\n"
        "v_pk_add_f32 v[220:221], v[232:233], v[234:235]\n"
        "v_pk_mul_f32 v[222:223], v[232:233], v[232:233]\n"
        "v_pk_add_f32 v[220:221], v[220:221], v[236:237]\n"
        "v_pk_fma_f32 v[222:223], v[234:235], v[234:235], v[222:223]\n"
        "v_pk_add_f32 v[220:221], v[220:221], v[238:239]\n"
        "v_pk_fma_f32 v[222:223], v[236:237], v[236:237], v[222:223]\n"
        "v_add_f32 v220, v220, v221\n"
        "v_pk_fma_f32 v[222:223], v[238:239], v[238:239], v[222:223]\n"
        "v_add_f32 v222, v222, v223\n"
        "s_add_u32 m0, s91, 0x840\n"
        "s_addk_i32 s93, 4096\n"
        "buffer_load_dwordx4 v13, s[8:11], s93 offen lds\n"
        "v_permlane32_swap_b32 v216, v218\n"
        "v_permlane32_swap_b32 v220, v222\n"
        "v_add_f32 v216, v216, v218\n"
        "v_add_f32 v220, v220, v222\n"
        "s_nop 1\n"
        "v_permlane16_swap_b32 v216, v220\n"
        "v_add_f32 v216, v216, v220\n"
        "s_nop 1\n"
        "v_add_f32_dpp v216, v216, v216 row_ror:8 row_mask:0xf bank_mask:0xf\n"
        "s_nop 1\n"
        "v_add_f32_dpp v216, v216, v216 row_ror:4 row_mask:0xf bank_mask:0xf\n"
        "s_nop 1\n"
        "v_add_f32_dpp v216, v216, v216 row_ror:2 row_mask:0xf bank_mask:0xf\n"
        "s_nop 1\n"
        "v_add_f32_dpp v216, v216, v216 row_ror:1 row_mask:0xf bank_mask:0xf\n"
        "v_mul_f32 v216, 0x3b000000, v216\n"
        "v_mov_b32 v217, v216\n"
        "s_nop 1\n"
        "v_permlane32_swap_b32 v216, v217\n"
        "v_fma_f32 v217, -v216, v216, v217\n"
        "v_max_f32 v217, 0, v217\n"
        "v_add_f32 v217, 0x3727c5ac, v217\n"
        "v_rsq_f32 v218, v217\n"
        "s_nop 0\n"
        "v_mul_f32 v219, v216, v218\n"
        "v_mov_b32 v220, v218\n"
        "v_mov_b32 v221, v219\n"
        "s_nop 0\n"
        "v_permlane16_swap_b32 v218, v220\n"
        "v_permlane16_swap_b32 v219, v221\n"
        "v_pk_fma_f32 v[224:225], v[224:225], v[218:219], v[218:219] op_sel:[0,0,1] op_sel_hi:[1,0,1] neg_lo:[0,0,1] neg_hi:[0,0,1]\n"
        "v_pk_fma_f32 v[226:227], v[226:227], v[218:219], v[218:219] op_sel:[0,0,1] op_sel_hi:[1,0,1] neg_lo:[0,0,1] neg_hi:[0,0,1]\n"
        "v_pk_fma_f32 v[228:229], v[228:229], v[218:219], v[218:219] op_sel:[0,0,1] op_sel_hi:[1,0,1] neg_lo:[0,0,1] neg_hi:[0,0,1]\n"
        "v_pk_fma_f32 v[230:231], v[230:231], v[218:219], v[218:219] op_sel:[0,0,1] op_sel_hi:[1,0,1] neg_lo:[0,0,1] neg_hi:[0,0,1]\n"
        "v_pk_fma_f32 v[232:233], v[232:233], v[220:221], v[220:221] op_sel:[0,0,1] op_sel_hi:[1,0,1] neg_lo:[0,0,1] neg_hi:[0,0,1]\n"
        "v_pk_fma_f32 v[234:235], v[234:235], v[220:221], v[220:221] op_sel:[0,0,1] op_sel_hi:[1,0,1] neg_lo:[0,0,1] neg_hi:[0,0,1]\n"
        "v_pk_fma_f32 v[236:237], v[236:237], v[220:221], v[220:221] op_sel:[0,0,1] op_sel_hi:[1,0,1] neg_lo:[0,0,1] neg_hi:[0,0,1]\n"
        "v_pk_fma_f32 v[238:239], v[238:239], v[220:221], v[220:221] op_sel:[0,0,1] op_sel_hi:[1,0,1] neg_lo:[0,0,1] neg_hi:[0,0,1]\n"
        "v_cvt_pk_bf16_f32 v244, v224, v225\n"
        "v_cvt_pk_bf16_f32 v245, v226, v227\n"
        "v_cvt_pk_bf16_f32 v246, v228, v229\n"
        "v_cvt_pk_bf16_f32 v247, v230, v231\n"
        "v_cvt_pk_bf16_f32 v248, v232, v233\n"
        "v_cvt_pk_bf16_f32 v249, v234, v235\n"
        "v_cvt_pk_bf16_f32 v250, v236, v237\n"
        "v_cvt_pk_bf16_f32 v251, v238, v239\n"
        "s_mul_i32 s55, s50, 0x880\n"
        "s_add_u32 s55, s55, 0x18c00\n"
        "v_add_u32 v6, s55, v2\n"
        "ds_write_b128 v6, v[244:247]\n"
        "ds_write_b128 v6, v[248:251] offset:1088\n"
        "s_add_u32 m0, s91, 0xc60\n"
        "s_addk_i32 s93, 4096\n"
        "buffer_load_dwordx4 v13, s[8:11], s93 offen lds\n"
        "s_waitcnt vmcnt(8)\n"
        "v_lshlrev_b32 v224, 16, v16\n"
        "v_and_b32 v225, 0xffff0000, v16\n"
        "v_mul_f32 v226, 0xbfb8aa3b, v224\n"
        "v_mul_f32 v227, 0xbfb8aa3b, v225\n"
        "v_exp_f32 v226, v226\n"
        "v_exp_f32 v227, v227\n"
        "v_add_f32 v226, 1.0, v226\n"
        "v_add_f32 v227, 1.0, v227\n"
        "v_rcp_f32 v226, v226\n"
        "v_rcp_f32 v227, v227\n"
        "v_mul_f32 v224, v224, v226\n"
        "v_mul_f32 v225, v225, v227\n"
        "v_cvt_pk_bf16_f32 v224, v224, v225\n"
        "s_lshl_b32 s55, s50, 8\n"
        "s_add_u32 s55, s55, 0x1b000\n"
        "v_add_u32 v6, s55, v14\n"
        "ds_write_b32 v6, v224\n"
        "v_readfirstlane_b32 s56, v240\n"
        "v_readfirstlane_b32 s57, v241\n"
        "v_mov_b32 v242, v240\n"
        "v_mov_b32 v243, v241\n"
        "s_add_u32 s56, s56, 0x7f4a7c15\n"
        "s_addc_u32 s57, s57, 0x9e3779b9\n"
        "v_bfe_u32 v17, v0, 2, 2\n"
        "v_and_b32 v18, 48, v2\n"
        "v_mul_u32_u24 v7, 1088, v17\n"
        "v_bfe_u32 v8, v0, 5, 1\n"
        "v_lshl_add_u32 v7, v8, 6, v7\n"
        "v_bfe_u32 v6, v0, 4, 1\n"
        "v_lshl_add_u32 v7, v6, 3, v7\n"
        "s_lshl_b32 s58, s50, 8\n"
        "s_add_u32 s58, s58, 0x18c00\n"
        "v_add3_u32 v7, v7, v18, s58\n"
        "v_mul_u32_u24 v230, 0x41000, v8\n"
        "v_lshl_add_u32 v230, v6, 11, v230\n"
        "v_lshl_add_u32 v230, v4, 4, v230\n"
        "v_add_u32 v228, s55, v3\n"
        "v_lshrrev_b32 v8, 1, v17\n"
        "v_lshlrev_b32 v8, 6, v8\n"
        "s_lshl_b32 s58, s50, 5\n"
        "v_xor_b32 v8, s58, v8\n"
        "v_lshl_add_u32 v8, v17, 7, v8\n"
        "v_lshl_add_u32 v8, v3, 6, v8\n"
        "v_lshrrev_b32 v229, 1, v18\n"
        "v_add_u32 v229, v229, v8\n"
        "v_add_u32 v229, 0x21000, v229\n"
        "s_waitcnt vmcnt(4) lgkmcnt(0)\n"
        "s_barrier\n"
        "ds_read_b64_tr_b16 v[244:245], v7\n"
        "ds_read_b64_tr_b16 v[246:247], v7 offset:4352\n"
        "ds_read_b64_tr_b16 v[248:249], v7 offset:128\n"
        "ds_read_b64_tr_b16 v[250:251], v7 offset:4480\n"
        "ds_read_b128 v[200:203], v228\n"
        "ds_read_b64_tr_b16 v[184:185], v229\n"
        "ds_read_b64_tr_b16 v[186:187], v229 offset:512\n"
        "ds_read_b128 v[204:207], v228 offset:64\n"
        "ds_read_b64_tr_b16 v[188:189], v229 offset:4096\n"
        "ds_read_b64_tr_b16 v[190:191], v229 offset:4608\n"
        "ds_read_b128 v[208:211], v228 offset:128\n"
        "ds_read_b64_tr_b16 v[192:193], v229 offset:8192\n"
        "ds_read_b64_tr_b16 v[194:195], v229 offset:8704\n"
        "ds_read_b128 v[212:215], v228 offset:192\n"
        "ds_read_b64_tr_b16 v[196:197], v229 offset:12288\n"
        "ds_read_b64_tr_b16 v[198:199], v229 offset:12800\n"
        "s_mul_i32 s55, s50, 0x104000\n"
        "s_lshr_b32 s58, s60, 2\n"
        "s_mul_i32 s58, s58, 0x1000\n"
        "s_add_u32 s55, s55, s58\n"
        "s_and_b32 s58, s60, 3\n"
        "s_mul_i32 s58, s58, 0x200\n"
        "s_add_u32 s55, s55, s58\n"
        "s_add_u32 s55, s55, 0x200000\n"
        "s_add_u32 s58, s55, 0x82000\n"
        "s_waitcnt lgkmcnt(12)\n"
        "buffer_store_dwordx4 v[244:247], v230, s[40:43], s55 offen sc1\n"
        "buffer_store_dwordx4 v[248:251], v230, s[40:43], s58 offen sc1\n"
        "s_waitcnt lgkmcnt(9)\n"
        "v_mfma_f32_16x16x32_bf16 v[216:219], v[200:203], v[184:187], 0\n"
        "s_waitcnt lgkmcnt(6)\n"
        "v_mfma_f32_16x16x32_bf16 v[216:219], v[204:207], v[188:191], v[216:219]\n"
        "s_waitcnt lgkmcnt(3)\n"
        "v_mfma_f32_16x16x32_bf16 v[216:219], v[208:211], v[192:195], v[216:219]\n"
        "s_waitcnt lgkmcnt(0)\n"
        "v_mfma_f32_16x16x32_bf16 v[216:219], v[212:215], v[196:199], v[216:219]\n"
        "v_cmp_gt_u32 vcc, 16, v1\n"
        "v_mov_b32 v6, 0x80000000\n"
        "s_nop 0\n"
        "v_cndmask_b32 v8, v6, v14, vcc\n"
        "s_mul_i32 s55, s61, 0x3000\n"
        "s_lshl_b32 s58, s62, 2\n"
        "s_add_u32 s58, s58, s50\n"
        "s_lshl_b32 s58, s58, 6\n"
        "s_add_u32 s55, s55, s58\n"
        "s_add_u32 s55, s55, 0x8000\n"
        "buffer_store_dword v216, v8, s[44:47], s55 offen sc1\n"
        "s_lshl_b32 s68, s51, 2\n"
        "s_add_u32 s68, s68, s50\n"
        "s_lshl_b32 s55, s68, 6\n"
        "s_lshl_b32 s65, s52, 14\n"
        "s_add_u32 s65, s65, 0x20000\n"
        "s_add_u32 s64, s55, s65\n"
        "v_cmp_gt_u32 vcc, 4, v1\n"
        "s_nop 1\n"
        "v_cndmask_b32 v19, v6, v18, vcc\n"
        "v_cmp_gt_u32 vcc, 4, v0\n"
        "s_nop 1\n"
        "v_cndmask_b32 v8, v6, v18, vcc\n"
        "v_mov_b32 v252, s56\n"
        "v_mov_b32 v253, s57\n"
        "v_mov_b32 v254, s56\n"
        "v_mov_b32 v255, s57\n"
        "s_add_u32 m0, s91, 0x1080\n"
        "s_addk_i32 s93, 4096\n"
        "buffer_load_dwordx4 v13, s[8:11], s93 offen lds\n"
        "s_add_u32 m0, s91, 0x14a0\n"
        "s_addk_i32 s93, 4096\n"
        "buffer_load_dwordx4 v13, s[8:11], s93 offen lds\n"
        "s_add_u32 m0, s91, 0x18c0\n"
        "s_addk_i32 s93, 4096\n"
        "buffer_load_dwordx4 v13, s[8:11], s93 offen lds\n"
        "s_lshl_b32 s69, s88, 2\n"
        "s_add_u32 s69, s69, s63\n"
        "s_lshl_b32 s55, s69, 2\n"
        "s_add_u32 s55, s55, s50\n"
        "s_lshl_b32 s55, s55, 6\n"
        "s_lshl_b32 s67, s89, 14\n"
        "s_add_u32 s67, s67, 0x20000\n"
        "s_add_u32 s66, s55, s67\n"
        "s_lshl_b32 s55, s60, 6\n"
        "s_add_u32 s55, s55, 0x1000\n"
        "s_waitcnt vmcnt(3)\n"
        "s_barrier\n"
        "buffer_store_dwordx4 v[252:255], v8, s[40:43], s55 offen sc1\n"
        "v_lshl_add_u32 v8, v17, 11, v18\n"
        "v_cmp_gt_u32 vcc, 12, v1\n"
        "s_add_u32 s55, s64, 0x1800\n"
        "s_nop 0\n"
        "v_cndmask_b32 v8, v6, v8, vcc\n"
        "buffer_store_dwordx4 v[240:243], v8, s[40:43], s66 offen sc1\n"
        "buffer_store_dwordx4 v[240:243], v8, s[40:43], s55 offen sc1\n"
        "s_and_b32 s29, s50, 1\n"
        "s_lshr_b32 s30, s50, 1\n"
        "v_lshlrev_b32 v6, 4, v4\n"
        "v_lshl_add_u32 v5, v3, 5, v6\n"
        "v_mul_u32_u24 v6, 1056, v4\n"
        "v_add_u32 v16, v6, v3\n"
        "v_add_u32 v22, 0x10800, v16\n"
        "s_mul_i32 s55, s30, 0x4200\n"
        "s_mul_i32 s58, s29, 0x8400\n"
        "s_add_u32 s55, s55, s58\n"
        "v_add_u32 v12, s55, v16\n"
        "s_add_u32 m0, s91, 0x1ce0\n"
        "s_addk_i32 s93, 4096\n"
        "buffer_load_dwordx4 v13, s[8:11], s93 offen lds\n"
        "v_mov_b32 v248, 0x3f803f80\n"
        "v_mov_b32 v249, 0x3f803f80\n"
        "v_mov_b32 v250, 0x3f803f80\n"
        "v_mov_b32 v251, 0x3f803f80\n"
        "v_mov_b32 v240, 0\n"
        "v_mov_b32 v241, 0\n"
        "v_mov_b32 v244, 0\n"
        "v_mov_b32 v245, 0\n"
        "v_bfe_u32 v6, v0, 4, 2\n"
        "v_cmp_eq_u32 vcc, v6, v17\n"
        "v_and_b32 v8, 16, v2\n"
        "v_mov_b32 v6, 0x3f80\n"
        "v_lshlrev_b32 v8, v8, v6\n"
        "v_cndmask_b32 v8, 0, v8, vcc\n"
        "v_and_b32 v6, 2, v0\n"
        "v_cmp_eq_u32 vcc, 0, v6\n"
        "s_add_u32 s94, s90, 0x0\n"
        "s_and_b32 s94, s94, 3\n"
        "s_lshl_b32 s94, s94, 9\n"
        "s_add_u32 s94, s94, s92\n"
        "s_add_u32 m0, s91, 0x8400\n"
        "s_nop 0\n"
        "buffer_load_dwordx4 v13, s[8:11], s94 offen lds\n"
        "v_cndmask_b32 v242, 0, v8, vcc\n"
        "v_xor_b32 v243, v242, v8\n"
        "s_lshl_b32 s55, s63, 2\n"
        "s_add_u32 s55, s55, s50\n"
        "s_mul_i32 s31, s55, 0x41000\n"
        "s_mul_i32 s58, s88, 0x20000\n"
        "s_add_u32 s31, s31, s58\n"
        "s_add_u32 s31, s31, 0x200000\n"
        "s_mul_i32 s34, s55, 0x1000\n"
        "s_lshl_b32 s58, s89, 2\n"
        "s_add_u32 s58, s58, s88\n"
        "s_lshl_b32 s61, s58, 10\n"
        "s_add_u32 s61, s61, 0xe0000\n"
        "s_mul_i32 s58, s58, 0x11000\n"
        "s_add_u32 s34, s34, s58\n"
        "s_add_u32 s34, s34, 0xc00000\n"
        "s_add_u32 s93, s90, 0x1\n"
        "s_and_b32 s93, s93, 3\n"
        "s_lshl_b32 s93, s93, 9\n"
        "s_add_u32 s93, s93, s92\n"
        "s_add_u32 m0, s91, 0x10800\n"
        "s_xor_b32 s93, s93, 0x20000\n"
        "buffer_load_dwordx4 v13, s[8:11], s93 offen lds\n"
        "s_lshl_b32 s62, s89, 3\n"
        "s_add_u32 s62, s62, s69\n"
        "s_lshl_b32 s62, s62, 2\n"
        "s_add_u32 s62, s62, s50\n"
        "s_mul_i32 s35, s62, 0x2000\n"
        "s_add_u32 s35, s35, 0x1500000\n"
        "s_waitcnt vmcnt(8)\n"
        "s_add_u32 m0, s91, 0x10c20\n"
        "s_addk_i32 s93, 4096\n"
        "buffer_load_dwordx4 v13, s[8:11], s93 offen lds\n"
        "s_lshl_b32 s71, s95, 2\n"
        "s_add_u32 s71, s71, s29\n"
        "s_lshl_b32 s71, s71, 2\n"
        "s_add_u32 s71, s71, s30\n"
        "s_lshl_b32 s71, s71, 12\n"
        "s_lshl_b32 s55, s63, 9\n"
        "s_cmp_eq_u32 s50, 3\n"
        "s_cselect_b32 s58, s69, s95\n"
        "s_lshl_b32 s58, s58, 8\n"
        "s_cmp_lt_u32 s50, 2\n"
        "s_cselect_b32 s72, s55, s58\n"
        "s_lshl_b32 s55, s50, 11\n"
        "s_add_u32 s72, s72, s55\n"
        "s_add_u32 s72, s72, 0x8000\n"
        "s_cmp_eq_u32 s50, 2\n"
        "s_cselect_b32 s55, 0x1700, 0\n"
        "s_cmp_eq_u32 s50, 3\n"
        "s_cselect_b32 s55, 0x700, s55\n"
        "v_bfe_u32 v7, v0, 4, 1\n"
        "v_and_b32 v6, 0x1f0, v2\n"
        "v_mad_u32_u24 v23, s55, v7, v6\n"
        "s_waitcnt vmcnt(8)\n"
        "s_add_u32 m0, s91, 0x8820\n"
        "s_addk_i32 s94, 4096\n"
        "buffer_load_dwordx4 v13, s[8:11], s94 offen lds\n"
        "v_and_b32 v6, 0xd0, v0\n"
        "v_cmp_eq_u32 vcc, 0xd0, v6\n"
        "s_cmp_eq_u32 s50, 1\n"
        "s_cselect_b32 s55, 1.0, 0\n"
        "v_mov_b32 v6, s55\n"
        "v_mov_b32 v8, 1.0\n"
        "v_cndmask_b32 v24, v6, v8, vcc\n"
        "v_xor_b32 v6, 1, v7\n"
        "v_lshlrev_b32 v6, 10, v6\n"
        "v_lshl_add_u32 v6, v4, 3, v6\n"
        "v_and_b32 v8, 0xe0, v0\n"
        "v_cmp_eq_u32 vcc, 0xc0, v8\n"
        "v_mov_b32 v8, 0x80000000\n"
        "s_lshl_b32 s73, s89, 4\n"
        "s_add_u32 s73, s73, s69\n"
        "v_cndmask_b32 v25, v8, v6, vcc\n"
        "s_lshl_b32 s73, s73, 7\n"
        "s_add_u32 s73, s73, 0x100000\n"
        "s_waitcnt vmcnt(8)\n"
        "s_add_u32 m0, s91, 0x11040\n"
        "s_addk_i32 s93, 4096\n"
        "buffer_load_dwordx4 v13, s[8:11], s93 offen lds\n"
        "v_and_b32 v6, 4, v11\n"
        "v_add_u32 v11, v11, v6\n"
        "v_mul_u32_u24 v26, 3584, v11\n"
        "v_add_u32 v26, v26, v10\n"
        "v_add_u32 v27, 0xdc00, v26\n"
        "v_lshl_add_u32 v28, v11, 10, v10\n"
        "v_add_u32 v29, 0x3c00, v28\n"
        "s_mul_i32 s74, s50, 0x1800\n"
        "s_add_u32 s74, s74, 0x21000\n"
        "s_mul_i32 s75, s50, 0x4200\n"
        "s_add_u32 s75, s75, 0x10800\n"
        "s_waitcnt vmcnt(8)\n"
        "s_add_u32 m0, s91, 0x11460\n"
        "s_addk_i32 s93, 4096\n"
        "buffer_load_dwordx4 v13, s[8:11], s93 offen lds\n"
        "v_lshrrev_b32 v6, 1, v17\n"
        "v_lshl_add_u32 v6, v6, 6, v18\n"
        "v_lshl_add_u32 v6, v17, 7, v6\n"
        "v_lshl_add_u32 v6, v3, 5, v6\n"
        "v_add_u32 v30, s74, v6\n"
        "v_add_u32 v32, s75, v6\n"
        "v_xor_b32 v31, 64, v30\n"
        "v_xor_b32 v33, 64, v32\n"
        "s_lshl_b32 s76, s68, 7\n"
        "s_mul_i32 s58, s68, 0x25\n"
        "s_lshr_b32 s58, s58, 8\n"
        "s_mul_i32 s59, s58, 0x7\n"
        "s_sub_u32 s59, s68, s59\n"
        "s_mul_i32 s59, s59, 0x2000\n"
        "s_mul_i32 s58, s58, 0x1d000\n"
        "s_add_u32 s78, s58, s59\n"
        "s_mul_i32 s58, s52, 0x74000\n"
        "s_add_u32 s58, s58, 0x1d00000\n"
        "s_add_u32 s78, s78, s58\n"
        "s_mul_i32 s79, s50, 0x1d000\n"
        "s_add_u32 s79, s79, s58\n"
        "s_mul_i32 s80, s50, 0x70000\n"
        "s_lshl_b32 s58, s95, 7\n"
        "s_add_u32 s80, s80, s58\n"
        "s_waitcnt vmcnt(8)\n"
        "s_add_u32 m0, s91, 0x8c40\n"
        "s_addk_i32 s94, 4096\n"
        "buffer_load_dwordx4 v13, s[8:11], s94 offen lds\n"
        "s_add_u32 s60, s53, 0x0\n"
        "s_sub_u32 s58, s60, 7\n"
        "s_cselect_b32 s60, s60, s58\n"
        "s_mul_i32 s60, s60, 0x2000\n"
        "s_waitcnt vmcnt(8)\n"
        "s_add_u32 m0, s91, 0x11880\n"
        "s_addk_i32 s93, 4096\n"
        "buffer_load_dwordx4 v13, s[8:11], s93 offen lds\n"
        "s_add_u32 s68, s53, 0x1\n"
        "s_sub_u32 s58, s68, 7\n"
        "s_cselect_b32 s68, s68, s58\n"
        "s_mul_i32 s68, s68, 0x2000\n"
        "s_waitcnt vmcnt(8)\n"
        "s_add_u32 m0, s91, 0x11ca0\n"
        "s_addk_i32 s93, 4096\n"
        "buffer_load_dwordx4 v13, s[8:11], s93 offen lds\n"
        "s_add_u32 s81, s53, 0x2\n"
        "s_sub_u32 s58, s81, 7\n"
        "s_cselect_b32 s81, s81, s58\n"
        "s_mul_i32 s81, s81, 0x2000\n"
        "s_waitcnt vmcnt(8)\n"
        "s_add_u32 m0, s91, 0x9060\n"
        "s_addk_i32 s94, 4096\n"
        "buffer_load_dwordx4 v13, s[8:11], s94 offen lds\n"
        "s_add_u32 s82, s53, 0x3\n"
        "s_sub_u32 s58, s82, 7\n"
        "s_cselect_b32 s82, s82, s58\n"
        "s_mul_i32 s82, s82, 0x2000\n"
        "s_waitcnt vmcnt(8)\n"
        "s_add_u32 m0, s91, 0x120c0\n"
        "s_addk_i32 s93, 4096\n"
        "buffer_load_dwordx4 v13, s[8:11], s93 offen lds\n"
        "s_add_u32 s83, s53, 0x4\n"
        "s_sub_u32 s58, s83, 7\n"
        "s_cselect_b32 s83, s83, s58\n"
        "s_mul_i32 s83, s83, 0x2000\n"
        "s_waitcnt vmcnt(8)\n"
        "s_add_u32 m0, s91, 0x124e0\n"
        "s_addk_i32 s93, 4096\n"
        "buffer_load_dwordx4 v13, s[8:11], s93 offen lds\n"
        "s_add_u32 s84, s53, 0x5\n"
        "s_sub_u32 s58, s84, 7\n"
        "s_cselect_b32 s84, s84, s58\n"
        "s_mul_i32 s84, s84, 0x2000\n"
        "s_waitcnt vmcnt(8)\n"
        "s_add_u32 m0, s91, 0x9480\n"
        "s_addk_i32 s94, 4096\n"
        "buffer_load_dwordx4 v13, s[8:11], s94 offen lds\n"
        "s_add_u32 s85, s53, 0x6\n"
        "s_sub_u32 s58, s85, 7\n"
        "s_cselect_b32 s85, s85, s58\n"
        "s_mul_i32 s85, s85, 0x2000\n"
        "s_waitcnt vmcnt(8)\n"
        "s_add_u32 s93, s90, 0x2\n"
        "s_and_b32 s93, s93, 3\n"
        "s_lshl_b32 s93, s93, 9\n"
        "s_add_u32 s93, s93, s92\n"
        "s_add_u32 m0, s91, 0x18c00\n"
        "s_xor_b32 s93, s93, 0x20000\n"
        "buffer_load_dwordx4 v13, s[8:11], s93 offen lds\n"
        "s_lshl_b32 s55, s50, 7\n"
        "s_add_u32 s55, s55, 0x27000\n"
        "v_lshl_add_u32 v10, v3, 1, s55\n"
        "v_lshlrev_b32 v11, 6, v0\n"
        "s_cmp_eq_u32 s29, 0\n"
        "s_getpc_b64 s[86:87]\n"
        ".Lkr0:\n"
        "s_cbranch_scc1 .Lks0\n"
        "s_add_u32 s86, s86, .Lkv0-.Lkr0\n"
        "s_addc_u32 s87, s87, 0\n"
        "s_cmp_eq_u32 s30, 0\n"
        "s_cbranch_scc1 .Lkt1\n"
        "s_branch .Lkt3\n"
        ".Lks0:\n"
        "s_add_u32 s86, s86, .Lkv0-.Lkr0\n"
        "s_addc_u32 s87, s87, 0\n"
        "s_cmp_eq_u32 s30, 0\n"
        "s_cbranch_scc1 .Lkt0\n"
        "s_branch .Lkt2\n"
        ".Lkv0:\n"
        "s_waitcnt vmcnt(8)\n"
        "s_add_u32 m0, s91, 0x19020\n"
        "s_addk_i32 s93, 4096\n"
        "buffer_load_dwordx4 v13, s[8:11], s93 offen lds\n"
        "s_waitcnt vmcnt(8)\n"
        "s_add_u32 m0, s91, 0x98a0\n"
        "s_addk_i32 s94, 4096\n"
        "buffer_load_dwordx4 v13, s[8:11], s94 offen lds\n"
        "s_cmp_eq_u32 s29, 0\n"
        "s_getpc_b64 s[86:87]\n"
        ".Lkr1:\n"
        "s_cbranch_scc1 .Lks1\n"
        "s_add_u32 s86, s86, .Lkv1-.Lkr1\n"
        "s_addc_u32 s87, s87, 0\n"
        "s_cmp_eq_u32 s30, 0\n"
        "s_cbranch_scc1 .Lkt5\n"
        "s_branch .Lkt7\n"
        ".Lks1:\n"
        "s_add_u32 s86, s86, .Lkv1-.Lkr1\n"
        "s_addc_u32 s87, s87, 0\n"
        "s_cmp_eq_u32 s30, 0\n"
        "s_cbranch_scc1 .Lkt4\n"
        "s_branch .Lkt6\n"
        ".Lkv1:\n"
        "s_waitcnt vmcnt(8)\n"
        "s_add_u32 m0, s91, 0x19440\n"
        "s_addk_i32 s93, 4096\n"
        "buffer_load_dwordx4 v13, s[8:11], s93 offen lds\n"
        "s_waitcnt vmcnt(8)\n"
        "s_add_u32 m0, s91, 0x19860\n"
        "s_addk_i32 s93, 4096\n"
        "buffer_load_dwordx4 v13, s[8:11], s93 offen lds\n"
        "s_mov_b32 s28, 0x1000\n"
        "buffer_load_dwordx2 v[20:21], v11, s[40:43], s28 offen sc1\n"
        "s_add_u32 m0, s91, 0x9cc0\n"
        "s_addk_i32 s94, 4096\n"
        "buffer_load_dwordx4 v13, s[8:11], s94 offen lds\n"
        "s_add_u32 m0, s91, 0x19c80\n"
        "s_addk_i32 s93, 4096\n"
        "buffer_load_dwordx4 v13, s[8:11], s93 offen lds\n"
        "s_add_u32 m0, s91, 0x1a0a0\n"
        "s_addk_i32 s93, 4096\n"
        "buffer_load_dwordx4 v13, s[8:11], s93 offen lds\n"
        "s_add_u32 m0, s91, 0xa0e0\n"
        "s_addk_i32 s94, 4096\n"
        "buffer_load_dwordx4 v13, s[8:11], s94 offen lds\n"
        "s_add_u32 m0, s91, 0x1a4c0\n"
        "s_addk_i32 s93, 4096\n"
        "buffer_load_dwordx4 v13, s[8:11], s93 offen lds\n"
        "s_add_u32 m0, s91, 0x1a8e0\n"
        "s_addk_i32 s93, 4096\n"
        "buffer_load_dwordx4 v13, s[8:11], s93 offen lds\n"
        "s_waitcnt vmcnt(6)\n"
        "v_cmp_ne_u64 vcc, s[56:57], v[20:21]\n"
        "s_nop 0\n"
        "s_cbranch_vccnz .Lkp0\n"
        ".Lkq0:\n"
        "s_barrier\n"
        "s_add_u32 s33, s90, 0x0\n"
        "s_and_b32 s33, s33, 3\n"
        "s_mul_i32 s33, s33, 0x8000\n"
        "s_add_u32 s33, s33, s31\n"
        "buffer_load_dwordx4 a[0:3], v5, s[40:43], s33 offen sc1\n"
        "buffer_load_dwordx4 a[4:7], v5, s[40:43], s33 offen offset:2048 sc1\n"
        "s_addk_i32 s33, 4096\n"
        "buffer_load_dwordx4 a[8:11], v5, s[40:43], s33 offen sc1\n"
        "buffer_load_dwordx4 a[12:15], v5, s[40:43], s33 offen offset:2048 sc1\n"
        "s_addk_i32 s33, 4096\n"
        "buffer_load_dwordx4 a[16:19], v5, s[40:43], s33 offen sc1\n"
        "buffer_load_dwordx4 a[20:23], v5, s[40:43], s33 offen offset:2048 sc1\n"
        "s_addk_i32 s33, 4096\n"
        "buffer_load_dwordx4 a[24:27], v5, s[40:43], s33 offen sc1\n"
        "buffer_load_dwordx4 a[28:31], v5, s[40:43], s33 offen offset:2048 sc1\n"
        "s_addk_i32 s33, 4096\n"
        "buffer_load_dwordx4 a[32:35], v5, s[40:43], s33 offen sc1\n"
        "buffer_load_dwordx4 a[36:39], v5, s[40:43], s33 offen offset:2048 sc1\n"
        "s_addk_i32 s33, 4096\n"
        "buffer_load_dwordx4 a[40:43], v5, s[40:43], s33 offen sc1\n"
        "buffer_load_dwordx4 a[44:47], v5, s[40:43], s33 offen offset:2048 sc1\n"
        "v_and_b32 v11, 7, v0\n"
        "s_lshl_b32 s55, s50, 9\n"
        "v_lshl_add_u32 v11, v11, 6, s55\n"
        "v_or_b32 v6, s2, v0\n"
        "v_cmp_eq_u32 vcc, 0, v6\n"
        "v_mov_b32 v7, 0x80000000\n"
        "s_nop 0\n"
        "v_cndmask_b32 v6, v7, v15, vcc\n"
        "buffer_store_dwordx2 v[252:253], v6, s[40:43], 0 offen sc1\n"
        "v_cmp_gt_u32 vcc, 32, v1\n"
        "s_nop 1\n"
        "v_cndmask_b32 v8, v7, v14, vcc\n"
        "s_waitcnt vmcnt(15)\n"
        "s_barrier\n"
        "ds_read_b128 v[116:119], v12\n"
        "ds_read_b128 v[120:123], v12 offset:512\n"
        "ds_read_b128 v[124:127], v12 offset:64\n"
        "ds_read_b128 v[128:131], v12 offset:576\n"
        "s_branch .Lku0\n"
        ".Lkt0:\n"
        "s_setpc_b64 s[86:87]\n"
        ".Lku0:\n"
        "ds_read_b128 v[132:135], v12 offset:128\n"
        "ds_read_b128 v[136:139], v12 offset:640\n"
        "ds_read_b128 v[140:143], v12 offset:192\n"
        "ds_read_b128 v[144:147], v12 offset:704\n"
        "s_waitcnt lgkmcnt(6)\n"
        "v_mfma_f32_16x16x32_bf16 v[108:111], v[248:251], v[116:119], 0\n"
        "ds_read_b128 v[148:151], v12 offset:256\n"
        "v_mfma_f32_16x16x32_bf16 v[112:115], v[248:251], v[120:123], 0\n"
        "ds_read_b128 v[152:155], v12 offset:768\n"
        "s_waitcnt lgkmcnt(6)\n"
        "v_mfma_f32_16x16x32_bf16 v[108:111], v[248:251], v[124:127], v[108:111]\n"
        "ds_read_b128 v[116:119], v12 offset:320\n"
        "v_mfma_f32_16x16x32_bf16 v[112:115], v[248:251], v[128:131], v[112:115]\n"
        "ds_read_b128 v[120:123], v12 offset:832\n"
        "s_waitcnt lgkmcnt(6)\n"
        "v_mfma_f32_16x16x32_bf16 v[108:111], v[248:251], v[132:135], v[108:111]\n"
        "ds_read_b128 v[124:127], v12 offset:384\n"
        "v_mfma_f32_16x16x32_bf16 v[112:115], v[248:251], v[136:139], v[112:115]\n"
        "ds_read_b128 v[128:131], v12 offset:896\n"
        "s_waitcnt lgkmcnt(6)\n"
        "v_mfma_f32_16x16x32_bf16 v[108:111], v[248:251], v[140:143], v[108:111]\n"
        "ds_read_b128 v[132:135], v12 offset:448\n"
        "v_mfma_f32_16x16x32_bf16 v[112:115], v[248:251], v[144:147], v[112:115]\n"
        "ds_read_b128 v[136:139], v12 offset:960\n"
        "s_waitcnt lgkmcnt(6)\n"
        "v_mfma_f32_16x16x32_bf16 v[108:111], v[248:251], v[148:151], v[108:111]\n"
        "ds_read_b128 v[140:143], v16\n"
        "v_mfma_f32_16x16x32_bf16 v[112:115], v[248:251], v[152:155], v[112:115]\n"
        "ds_read_b128 v[144:147], v16 offset:512\n"
        "s_waitcnt lgkmcnt(6)\n"
        "v_mfma_f32_16x16x32_bf16 v[108:111], v[248:251], v[116:119], v[108:111]\n"
        "ds_read_b128 v[148:151], v16 offset:16896\n"
        "v_mfma_f32_16x16x32_bf16 v[112:115], v[248:251], v[120:123], v[112:115]\n"
        "ds_read_b128 v[152:155], v16 offset:17408\n"
        "s_waitcnt lgkmcnt(6)\n"
        "v_mfma_f32_16x16x32_bf16 v[108:111], v[248:251], v[124:127], v[108:111]\n"
        "ds_read_b128 v[116:119], v16 offset:64\n"
        "v_mfma_f32_16x16x32_bf16 v[112:115], v[248:251], v[128:131], v[112:115]\n"
        "ds_read_b128 v[120:123], v16 offset:576\n"
        "s_waitcnt lgkmcnt(6)\n"
        "v_mfma_f32_16x16x32_bf16 v[108:111], v[248:251], v[132:135], v[108:111]\n"
        "ds_read_b128 v[124:127], v16 offset:16960\n"
        "v_mfma_f32_16x16x32_bf16 v[112:115], v[248:251], v[136:139], v[112:115]\n"
        "ds_read_b128 v[128:131], v16 offset:17472\n"
        "s_waitcnt vmcnt(11) lgkmcnt(6)\n"
        "v_mfma_f32_16x16x32_bf16 v[156:159], a[0:3], v[140:143], 0\n"
        "ds_read_b128 v[132:135], v16 offset:128\n"
        "v_mfma_f32_16x16x32_bf16 v[160:163], a[4:7], v[140:143], 0\n"
        "v_mfma_f32_16x16x32_bf16 v[164:167], a[0:3], v[144:147], 0\n"
        "ds_read_b128 v[136:139], v16 offset:640\n"
        "v_and_b32 v6, 16, v0\n"
        "v_mfma_f32_16x16x32_bf16 v[168:171], a[4:7], v[144:147], 0\n"
        "v_cmp_eq_u32 vcc, 0, v6\n"
        "s_xor_b32 s59, s50, s88\n"
        "s_waitcnt lgkmcnt(6)\n"
        "v_mfma_f32_16x16x32_bf16 v[188:191], a[0:3], v[148:151], 0\n"
        "ds_read_b128 v[140:143], v16 offset:17024\n"
        "s_xor_b32 s59, s59, 1\n"
        "v_mfma_f32_16x16x32_bf16 v[192:195], a[4:7], v[148:151], 0\n"
        "s_sub_u32 s59, s59, s88\n"
        "v_cndmask_b32 v108, v112, v108, vcc\n"
        "v_mfma_f32_16x16x32_bf16 v[196:199], a[0:3], v[152:155], 0\n"
        "ds_read_b128 v[144:147], v16 offset:17536\n"
        "s_lshl_b32 s59, s59, 3\n"
        "v_mfma_f32_16x16x32_bf16 v[200:203], a[4:7], v[152:155], 0\n"
        "s_add_u32 s59, s59, s69\n"
        "s_lshl_b32 s59, s59, 7\n"
        "s_waitcnt vmcnt(9) lgkmcnt(6)\n"
        "v_mfma_f32_16x16x32_bf16 v[156:159], a[8:11], v[116:119], v[156:159]\n"
        "ds_read_b128 v[148:151], v16 offset:192\n"
        "s_add_u32 s59, s59, s61\n"
        "v_mfma_f32_16x16x32_bf16 v[160:163], a[12:15], v[116:119], v[160:163]\n"
        "buffer_store_dword v108, v8, s[40:43], s59 offen sc1\n"
        "s_addk_i32 s33, 4096\n"
        "v_mfma_f32_16x16x32_bf16 v[164:167], a[8:11], v[120:123], v[164:167]\n"
        "ds_read_b128 v[152:155], v16 offset:704\n"
        "buffer_load_dwordx4 a[48:51], v5, s[40:43], s33 offen sc1\n"
        "v_mfma_f32_16x16x32_bf16 v[168:171], a[12:15], v[120:123], v[168:171]\n"
        "buffer_load_dwordx4 a[52:55], v5, s[40:43], s33 offen offset:2048 sc1\n"
        "s_waitcnt lgkmcnt(6)\n"
        "v_mfma_f32_16x16x32_bf16 v[188:191], a[8:11], v[124:127], v[188:191]\n"
        "ds_read_b128 v[116:119], v16 offset:17088\n"
        "s_addk_i32 s33, 4096\n"
        "v_mfma_f32_16x16x32_bf16 v[192:195], a[12:15], v[124:127], v[192:195]\n"
        "buffer_load_dwordx4 a[56:59], v5, s[40:43], s33 offen sc1\n"
        "buffer_load_dwordx4 a[60:63], v5, s[40:43], s33 offen offset:2048 sc1\n"
        "v_mfma_f32_16x16x32_bf16 v[196:199], a[8:11], v[128:131], v[196:199]\n"
        "ds_read_b128 v[120:123], v16 offset:17600\n"
        "v_mfma_f32_16x16x32_bf16 v[200:203], a[12:15], v[128:131], v[200:203]\n"
        "s_waitcnt vmcnt(12) lgkmcnt(6)\n"
        "v_mfma_f32_16x16x32_bf16 v[156:159], a[16:19], v[132:135], v[156:159]\n"
        "ds_read_b128 v[124:127], v16 offset:256\n"
        "s_add_u32 s33, s90, 0x1\n"
        "v_mfma_f32_16x16x32_bf16 v[160:163], a[20:23], v[132:135], v[160:163]\n"
        "s_and_b32 s33, s33, 3\n"
        "s_mul_i32 s33, s33, 0x8000\n"
        "v_mfma_f32_16x16x32_bf16 v[164:167], a[16:19], v[136:139], v[164:167]\n"
        "ds_read_b128 v[128:131], v16 offset:768\n"
        "s_add_u32 s33, s33, s31\n"
        "v_mfma_f32_16x16x32_bf16 v[168:171], a[20:23], v[136:139], v[168:171]\n"
        "buffer_load_dwordx4 a[64:67], v5, s[40:43], s33 offen sc1\n"
        "buffer_load_dwordx4 a[68:71], v5, s[40:43], s33 offen offset:2048 sc1\n"
        "s_waitcnt lgkmcnt(6)\n"
        "v_mfma_f32_16x16x32_bf16 v[188:191], a[16:19], v[140:143], v[188:191]\n"
        "ds_read_b128 v[132:135], v16 offset:17152\n"
        "v_mfma_f32_16x16x32_bf16 v[192:195], a[20:23], v[140:143], v[192:195]\n"
        "v_mfma_f32_16x16x32_bf16 v[196:199], a[16:19], v[144:147], v[196:199]\n"
        "ds_read_b128 v[136:139], v16 offset:17664\n"
        "s_addk_i32 s33, 4096\n"
        "v_mfma_f32_16x16x32_bf16 v[200:203], a[20:23], v[144:147], v[200:203]\n"
        "buffer_load_dwordx4 a[72:75], v5, s[40:43], s33 offen sc1\n"
        "s_waitcnt vmcnt(13) lgkmcnt(6)\n"
        "v_mfma_f32_16x16x32_bf16 v[156:159], a[24:27], v[148:151], v[156:159]\n"
        "ds_read_b128 v[140:143], v16 offset:320\n"
        "v_mfma_f32_16x16x32_bf16 v[160:163], a[28:31], v[148:151], v[160:163]\n"
        "buffer_load_dwordx4 a[76:79], v5, s[40:43], s33 offen offset:2048 sc1\n"
        "v_mfma_f32_16x16x32_bf16 v[164:167], a[24:27], v[152:155], v[164:167]\n"
        "ds_read_b128 v[144:147], v16 offset:832\n"
        "v_mfma_f32_16x16x32_bf16 v[168:171], a[28:31], v[152:155], v[168:171]\n"
        "s_waitcnt lgkmcnt(6)\n"
        "v_mfma_f32_16x16x32_bf16 v[188:191], a[24:27], v[116:119], v[188:191]\n"
        "ds_read_b128 v[148:151], v16 offset:17216\n"
        "v_mfma_f32_16x16x32_bf16 v[192:195], a[28:31], v[116:119], v[192:195]\n"
        "s_addk_i32 s33, 4096\n"
        "buffer_load_dwordx4 a[80:83], v5, s[40:43], s33 offen sc1\n"
        "v_mfma_f32_16x16x32_bf16 v[196:199], a[24:27], v[120:123], v[196:199]\n"
        "ds_read_b128 v[152:155], v16 offset:17728\n"
        "v_mfma_f32_16x16x32_bf16 v[200:203], a[28:31], v[120:123], v[200:203]\n"
        "s_waitcnt vmcnt(13) lgkmcnt(6)\n"
        "v_mfma_f32_16x16x32_bf16 v[156:159], a[32:35], v[124:127], v[156:159]\n"
        "ds_read_b128 v[116:119], v16 offset:384\n"
        "buffer_load_dwordx4 a[84:87], v5, s[40:43], s33 offen offset:2048 sc1\n"
        "v_mfma_f32_16x16x32_bf16 v[160:163], a[36:39], v[124:127], v[160:163]\n"
        "v_mfma_f32_16x16x32_bf16 v[164:167], a[32:35], v[128:131], v[164:167]\n"
        "ds_read_b128 v[120:123], v16 offset:896\n"
        "v_mfma_f32_16x16x32_bf16 v[168:171], a[36:39], v[128:131], v[168:171]\n"
        "s_addk_i32 s33, 4096\n"
        "buffer_load_dwordx4 a[88:91], v5, s[40:43], s33 offen sc1\n"
        "s_waitcnt lgkmcnt(6)\n"
        "v_mfma_f32_16x16x32_bf16 v[188:191], a[32:35], v[132:135], v[188:191]\n"
        "ds_read_b128 v[124:127], v16 offset:17280\n"
        "v_mfma_f32_16x16x32_bf16 v[192:195], a[36:39], v[132:135], v[192:195]\n"
        "v_mfma_f32_16x16x32_bf16 v[196:199], a[32:35], v[136:139], v[196:199]\n"
        "ds_read_b128 v[128:131], v16 offset:17792\n"
        "buffer_load_dwordx4 a[92:95], v5, s[40:43], s33 offen offset:2048 sc1\n"
        "v_mfma_f32_16x16x32_bf16 v[200:203], a[36:39], v[136:139], v[200:203]\n"
        "s_waitcnt vmcnt(14) lgkmcnt(6)\n"
        "v_mfma_f32_16x16x32_bf16 v[156:159], a[40:43], v[140:143], v[156:159]\n"
        "ds_read_b128 v[132:135], v16 offset:448\n"
        "v_mfma_f32_16x16x32_bf16 v[160:163], a[44:47], v[140:143], v[160:163]\n"
        "s_addk_i32 s33, 4096\n"
        "buffer_load_dwordx4 a[96:99], v5, s[40:43], s33 offen sc1\n"
        "v_mfma_f32_16x16x32_bf16 v[164:167], a[40:43], v[144:147], v[164:167]\n"
        "ds_read_b128 v[136:139], v16 offset:960\n"
        "v_mfma_f32_16x16x32_bf16 v[168:171], a[44:47], v[144:147], v[168:171]\n"
        "s_waitcnt lgkmcnt(6)\n"
        "v_mfma_f32_16x16x32_bf16 v[188:191], a[40:43], v[148:151], v[188:191]\n"
        "ds_read_b128 v[140:143], v16 offset:17344\n"
        "v_mfma_f32_16x16x32_bf16 v[192:195], a[44:47], v[148:151], v[192:195]\n"
        "buffer_load_dwordx4 a[100:103], v5, s[40:43], s33 offen offset:2048 sc1\n"
        "v_mfma_f32_16x16x32_bf16 v[196:199], a[40:43], v[152:155], v[196:199]\n"
        "ds_read_b128 v[144:147], v16 offset:17856\n"
        "v_mfma_f32_16x16x32_bf16 v[200:203], a[44:47], v[152:155], v[200:203]\n"
        "s_waitcnt vmcnt(12) lgkmcnt(6)\n"
        "v_mfma_f32_16x16x32_bf16 v[156:159], a[48:51], v[116:119], v[156:159]\n"
        "s_addk_i32 s33, 4096\n"
        "buffer_load_dwordx4 a[104:107], v5, s[40:43], s33 offen sc1\n"
        "v_mfma_f32_16x16x32_bf16 v[160:163], a[52:55], v[116:119], v[160:163]\n"
        "v_mfma_f32_16x16x32_bf16 v[164:167], a[48:51], v[120:123], v[164:167]\n"
        "v_mfma_f32_16x16x32_bf16 v[168:171], a[52:55], v[120:123], v[168:171]\n"
        "buffer_load_dwordx4 a[108:111], v5, s[40:43], s33 offen offset:2048 sc1\n"
        "s_waitcnt lgkmcnt(4)\n"
        "v_mfma_f32_16x16x32_bf16 v[188:191], a[48:51], v[124:127], v[188:191]\n"
        "v_mfma_f32_16x16x32_bf16 v[192:195], a[52:55], v[124:127], v[192:195]\n"
        "s_waitcnt lgkmcnt(0)\n"
        "s_barrier\n"
        "v_mfma_f32_16x16x32_bf16 v[196:199], a[48:51], v[128:131], v[196:199]\n"
        "ds_read_b128 v[148:151], v22\n"
        "s_addk_i32 s33, 4096\n"
        "v_mfma_f32_16x16x32_bf16 v[200:203], a[52:55], v[128:131], v[200:203]\n"
        "ds_read_b128 v[152:155], v22 offset:512\n"
        "buffer_load_dwordx4 a[112:115], v5, s[40:43], s33 offen sc1\n"
        "s_waitcnt vmcnt(13)\n"
        "v_mfma_f32_16x16x32_bf16 v[156:159], a[56:59], v[132:135], v[156:159]\n"
        "ds_read_b128 v[116:119], v22 offset:16896\n"
        "v_mfma_f32_16x16x32_bf16 v[160:163], a[60:63], v[132:135], v[160:163]\n"
        "ds_read_b128 v[120:123], v22 offset:17408\n"
        "buffer_load_dwordx4 a[116:119], v5, s[40:43], s33 offen offset:2048 sc1\n"
        "v_mfma_f32_16x16x32_bf16 v[164:167], a[56:59], v[136:139], v[164:167]\n"
        "ds_read_b128 v[124:127], v22 offset:64\n"
        "v_mfma_f32_16x16x32_bf16 v[168:171], a[60:63], v[136:139], v[168:171]\n"
        "ds_read_b128 v[128:131], v22 offset:576\n"
        "v_mfma_f32_16x16x32_bf16 v[188:191], a[56:59], v[140:143], v[188:191]\n"
        "ds_read_b128 v[132:135], v22 offset:16960\n"
        "v_mfma_f32_16x16x32_bf16 v[192:195], a[60:63], v[140:143], v[192:195]\n"
        "s_addk_i32 s33, 4096\n"
        "buffer_load_dwordx4 a[120:123], v5, s[40:43], s33 offen sc1\n"
        "v_mfma_f32_16x16x32_bf16 v[196:199], a[56:59], v[144:147], v[196:199]\n"
        "ds_read_b128 v[136:139], v22 offset:17472\n"
        "v_mfma_f32_16x16x32_bf16 v[200:203], a[60:63], v[144:147], v[200:203]\n"
        "s_waitcnt vmcnt(13) lgkmcnt(6)\n"
        "v_mfma_f32_16x16x32_bf16 v[156:159], a[64:67], v[148:151], v[156:159]\n"
        "ds_read_b128 v[140:143], v22 offset:128\n"
        "buffer_load_dwordx4 a[124:127], v5, s[40:43], s33 offen offset:2048 sc1\n"
        "v_mfma_f32_16x16x32_bf16 v[160:163], a[68:71], v[148:151], v[160:163]\n"
        "v_mfma_f32_16x16x32_bf16 v[164:167], a[64:67], v[152:155], v[164:167]\n"
        "ds_read_b128 v[144:147], v22 offset:640\n"
        "v_mfma_f32_16x16x32_bf16 v[168:171], a[68:71], v[152:155], v[168:171]\n"
        "s_add_u32 s33, s90, 0x2\n"
        "s_and_b32 s33, s33, 3\n"
        "s_waitcnt lgkmcnt(6)\n"
        "v_mfma_f32_16x16x32_bf16 v[188:191], a[64:67], v[116:119], v[188:191]\n"
        "ds_read_b128 v[148:151], v22 offset:17024\n"
        "s_mul_i32 s33, s33, 0x8000\n"
        "v_mfma_f32_16x16x32_bf16 v[192:195], a[68:71], v[116:119], v[192:195]\n"
        "s_add_u32 s33, s33, s31\n"
        "buffer_load_dwordx4 a[128:131], v5, s[40:43], s33 offen sc1\n"
        "v_mfma_f32_16x16x32_bf16 v[196:199], a[64:67], v[120:123], v[196:199]\n"
        "ds_read_b128 v[152:155], v22 offset:17536\n"
        "buffer_load_dwordx4 a[132:135], v5, s[40:43], s33 offen offset:2048 sc1\n"
        "v_mfma_f32_16x16x32_bf16 v[200:203], a[68:71], v[120:123], v[200:203]\n"
        "s_waitcnt vmcnt(14) lgkmcnt(6)\n"
        "v_mfma_f32_16x16x32_bf16 v[156:159], a[72:75], v[124:127], v[156:159]\n"
        "ds_read_b128 v[116:119], v22 offset:192\n"
        "v_mfma_f32_16x16x32_bf16 v[160:163], a[76:79], v[124:127], v[160:163]\n"
        "s_addk_i32 s33, 4096\n"
        "buffer_load_dwordx4 a[136:139], v5, s[40:43], s33 offen sc1\n"
        "v_mfma_f32_16x16x32_bf16 v[164:167], a[72:75], v[128:131], v[164:167]\n"
        "ds_read_b128 v[120:123], v22 offset:704\n"
        "v_mfma_f32_16x16x32_bf16 v[168:171], a[76:79], v[128:131], v[168:171]\n"
        "s_waitcnt lgkmcnt(6)\n"
        "v_mfma_f32_16x16x32_bf16 v[188:191], a[72:75], v[132:135], v[188:191]\n"
        "ds_read_b128 v[124:127], v22 offset:17088\n"
        "v_mfma_f32_16x16x32_bf16 v[192:195], a[76:79], v[132:135], v[192:195]\n"
        "buffer_load_dwordx4 a[140:143], v5, s[40:43], s33 offen offset:2048 sc1\n"
        "v_mfma_f32_16x16x32_bf16 v[196:199], a[72:75], v[136:139], v[196:199]\n"
        "ds_read_b128 v[128:131], v22 offset:17600\n"
        "v_mfma_f32_16x16x32_bf16 v[200:203], a[76:79], v[136:139], v[200:203]\n"
        "s_waitcnt vmcnt(14) lgkmcnt(6)\n"
        "v_mfma_f32_16x16x32_bf16 v[156:159], a[80:83], v[140:143], v[156:159]\n"
        "ds_read_b128 v[132:135], v22 offset:256\n"
        "s_addk_i32 s33, 4096\n"
        "v_mfma_f32_16x16x32_bf16 v[160:163], a[84:87], v[140:143], v[160:163]\n"
        "buffer_load_dwordx4 a[144:147], v5, s[40:43], s33 offen sc1\n"
        "s_add_u32 s59, s66, 0x1000\n"
        "v_mfma_f32_16x16x32_bf16 v[164:167], a[80:83], v[144:147], v[164:167]\n"
        "ds_read_b128 v[136:139], v22 offset:768\n"
        "buffer_store_dwordx4 v[252:255], v19, s[40:43], s59 offen sc1\n"
        "v_mfma_f32_16x16x32_bf16 v[168:171], a[84:87], v[144:147], v[168:171]\n"
        "buffer_load_dwordx4 a[148:151], v5, s[40:43], s33 offen offset:2048 sc1\n"
        "s_waitcnt lgkmcnt(6)\n"
        "v_mfma_f32_16x16x32_bf16 v[188:191], a[80:83], v[148:151], v[188:191]\n"
        "ds_read_b128 v[140:143], v22 offset:17152\n"
        "v_mfma_f32_16x16x32_bf16 v[192:195], a[84:87], v[148:151], v[192:195]\n"
        "v_mfma_f32_16x16x32_bf16 v[196:199], a[80:83], v[152:155], v[196:199]\n"
        "ds_read_b128 v[144:147], v22 offset:17664\n"
        "s_addk_i32 s33, 4096\n"
        "v_mfma_f32_16x16x32_bf16 v[200:203], a[84:87], v[152:155], v[200:203]\n"
        "buffer_load_dwordx4 a[152:155], v5, s[40:43], s33 offen sc1\n"
        "s_waitcnt vmcnt(16) lgkmcnt(6)\n"
        "v_mfma_f32_16x16x32_bf16 v[156:159], a[88:91], v[116:119], v[156:159]\n"
        "ds_read_b128 v[148:151], v22 offset:320\n"
        "v_mfma_f32_16x16x32_bf16 v[160:163], a[92:95], v[116:119], v[160:163]\n"
        "buffer_load_dwordx4 a[156:159], v5, s[40:43], s33 offen offset:2048 sc1\n"
        "v_mfma_f32_16x16x32_bf16 v[164:167], a[88:91], v[120:123], v[164:167]\n"
        "ds_read_b128 v[152:155], v22 offset:832\n"
        "v_mfma_f32_16x16x32_bf16 v[168:171], a[92:95], v[120:123], v[168:171]\n"
        "s_waitcnt lgkmcnt(6)\n"
        "v_mfma_f32_16x16x32_bf16 v[188:191], a[88:91], v[124:127], v[188:191]\n"
        "ds_read_b128 v[116:119], v22 offset:17216\n"
        "v_mfma_f32_16x16x32_bf16 v[192:195], a[92:95], v[124:127], v[192:195]\n"
        "s_addk_i32 s33, 4096\n"
        "buffer_load_dwordx4 a[160:163], v5, s[40:43], s33 offen sc1\n"
        "v_mfma_f32_16x16x32_bf16 v[196:199], a[88:91], v[128:131], v[196:199]\n"
        "ds_read_b128 v[120:123], v22 offset:17728\n"
        "v_mfma_f32_16x16x32_bf16 v[200:203], a[92:95], v[128:131], v[200:203]\n"
        "s_waitcnt vmcnt(16) lgkmcnt(6)\n"
        "v_mfma_f32_16x16x32_bf16 v[156:159], a[96:99], v[132:135], v[156:159]\n"
        "ds_read_b128 v[124:127], v22 offset:384\n"
        "buffer_load_dwordx4 a[164:167], v5, s[40:43], s33 offen offset:2048 sc1\n"
        "v_mfma_f32_16x16x32_bf16 v[160:163], a[100:103], v[132:135], v[160:163]\n"
        "v_mfma_f32_16x16x32_bf16 v[164:167], a[96:99], v[136:139], v[164:167]\n"
        "ds_read_b128 v[128:131], v22 offset:896\n"
        "v_mfma_f32_16x16x32_bf16 v[168:171], a[100:103], v[136:139], v[168:171]\n"
        "s_addk_i32 s33, 4096\n"
        "buffer_load_dwordx4 a[168:171], v5, s[40:43], s33 offen sc1\n"
        "s_waitcnt lgkmcnt(6)\n"
        "v_mfma_f32_16x16x32_bf16 v[188:191], a[96:99], v[140:143], v[188:191]\n"
        "ds_read_b128 v[132:135], v22 offset:17280\n"
        "v_mfma_f32_16x16x32_bf16 v[192:195], a[100:103], v[140:143], v[192:195]\n"
        "v_mfma_f32_16x16x32_bf16 v[196:199], a[96:99], v[144:147], v[196:199]\n"
        "ds_read_b128 v[136:139], v22 offset:17792\n"
        "buffer_load_dwordx4 a[172:175], v5, s[40:43], s33 offen offset:2048 sc1\n"
        "v_mfma_f32_16x16x32_bf16 v[200:203], a[100:103], v[144:147], v[200:203]\n"
        "s_waitcnt vmcnt(17) lgkmcnt(6)\n"
        "v_mfma_f32_16x16x32_bf16 v[156:159], a[104:107], v[148:151], v[156:159]\n"
        "ds_read_b128 v[140:143], v22 offset:448\n"
        "v_mfma_f32_16x16x32_bf16 v[160:163], a[108:111], v[148:151], v[160:163]\n"
        "s_addk_i32 s33, 4096\n"
        "buffer_load_dwordx4 a[176:179], v5, s[40:43], s33 offen sc1\n"
        "v_mfma_f32_16x16x32_bf16 v[164:167], a[104:107], v[152:155], v[164:167]\n"
        "ds_read_b128 v[144:147], v22 offset:960\n"
        "v_mfma_f32_16x16x32_bf16 v[168:171], a[108:111], v[152:155], v[168:171]\n"
        "s_waitcnt lgkmcnt(6)\n"
        "v_mfma_f32_16x16x32_bf16 v[188:191], a[104:107], v[116:119], v[188:191]\n"
        "ds_read_b128 v[148:151], v22 offset:17344\n"
        "v_mfma_f32_16x16x32_bf16 v[192:195], a[108:111], v[116:119], v[192:195]\n"
        "buffer_load_dwordx4 a[180:183], v5, s[40:43], s33 offen offset:2048 sc1\n"
        "v_mfma_f32_16x16x32_bf16 v[196:199], a[104:107], v[120:123], v[196:199]\n"
        "ds_read_b128 v[152:155], v22 offset:17856\n"
        "v_mfma_f32_16x16x32_bf16 v[200:203], a[108:111], v[120:123], v[200:203]\n"
        "s_waitcnt vmcnt(17) lgkmcnt(6)\n"
        "v_mfma_f32_16x16x32_bf16 v[156:159], a[112:115], v[124:127], v[156:159]\n"
        "s_addk_i32 s33, 4096\n"
        "buffer_load_dwordx4 a[184:187], v5, s[40:43], s33 offen sc1\n"
        "v_mfma_f32_16x16x32_bf16 v[160:163], a[116:119], v[124:127], v[160:163]\n"
        "v_mfma_f32_16x16x32_bf16 v[164:167], a[112:115], v[128:131], v[164:167]\n"
        "v_mfma_f32_16x16x32_bf16 v[168:171], a[116:119], v[128:131], v[168:171]\n"
        "buffer_load_dwordx4 a[188:191], v5, s[40:43], s33 offen offset:2048 sc1\n"
        "s_waitcnt lgkmcnt(4)\n"
        "v_mfma_f32_16x16x32_bf16 v[188:191], a[112:115], v[132:135], v[188:191]\n"
        "v_mfma_f32_16x16x32_bf16 v[192:195], a[116:119], v[132:135], v[192:195]\n"
        "s_waitcnt lgkmcnt(0)\n"
        "s_barrier\n"
        "v_mfma_f32_16x16x32_bf16 v[196:199], a[112:115], v[136:139], v[196:199]\n"
        "ds_read_b128 v[116:119], v22 offset:33792\n"
        "s_add_u32 s93, s90, 0x3\n"
        "v_mfma_f32_16x16x32_bf16 v[200:203], a[116:119], v[136:139], v[200:203]\n"
        "ds_read_b128 v[120:123], v22 offset:34304\n"
        "s_and_b32 s93, s93, 3\n"
        "s_waitcnt vmcnt(17)\n"
        "v_mfma_f32_16x16x32_bf16 v[156:159], a[120:123], v[140:143], v[156:159]\n"
        "ds_read_b128 v[124:127], v22 offset:50688\n"
        "s_lshl_b32 s93, s93, 9\n"
        "v_mfma_f32_16x16x32_bf16 v[160:163], a[124:127], v[140:143], v[160:163]\n"
        "ds_read_b128 v[128:131], v22 offset:51200\n"
        "s_add_u32 s93, s93, s92\n"
        "v_mfma_f32_16x16x32_bf16 v[164:167], a[120:123], v[144:147], v[164:167]\n"
        "ds_read_b128 v[132:135], v22 offset:33856\n"
        "s_add_u32 m0, s91, 0x0\n"
        "v_mfma_f32_16x16x32_bf16 v[168:171], a[124:127], v[144:147], v[168:171]\n"
        "ds_read_b128 v[136:139], v22 offset:34368\n"
        "s_xor_b32 s93, s93, 0x20000\n"
        "v_mfma_f32_16x16x32_bf16 v[188:191], a[120:123], v[148:151], v[188:191]\n"
        "ds_read_b128 v[140:143], v22 offset:50752\n"
        "buffer_load_dwordx4 v13, s[8:11], s93 offen lds\n"
        "v_mfma_f32_16x16x32_bf16 v[192:195], a[124:127], v[148:151], v[192:195]\n"
        "s_add_u32 m0, s91, 0x420\n"
        "s_addk_i32 s93, 4096\n"
        "v_mfma_f32_16x16x32_bf16 v[196:199], a[120:123], v[152:155], v[196:199]\n"
        "ds_read_b128 v[144:147], v22 offset:51264\n"
        "buffer_load_dwordx4 v13, s[8:11], s93 offen lds\n"
        "v_mfma_f32_16x16x32_bf16 v[200:203], a[124:127], v[152:155], v[200:203]\n"
        "s_add_u32 m0, s91, 0x840\n"
        "s_addk_i32 s93, 4096\n"
        "s_waitcnt vmcnt(17) lgkmcnt(6)\n"
        "v_mfma_f32_16x16x32_bf16 v[156:159], a[128:131], v[116:119], v[156:159]\n"
        "ds_read_b128 v[148:151], v22 offset:33920\n"
        "buffer_load_dwordx4 v13, s[8:11], s93 offen lds\n"
        "v_mfma_f32_16x16x32_bf16 v[160:163], a[132:135], v[116:119], v[160:163]\n"
        "s_add_u32 m0, s91, 0xc60\n"
        "s_addk_i32 s93, 4096\n"
        "v_mfma_f32_16x16x32_bf16 v[164:167], a[128:131], v[120:123], v[164:167]\n"
        "ds_read_b128 v[152:155], v22 offset:34432\n"
        "buffer_load_dwordx4 v13, s[8:11], s93 offen lds\n"
        "v_mfma_f32_16x16x32_bf16 v[168:171], a[132:135], v[120:123], v[168:171]\n"
        "s_add_u32 m0, s91, 0x1080\n"
        "s_addk_i32 s93, 4096\n"
        "s_waitcnt lgkmcnt(6)\n"
        "v_mfma_f32_16x16x32_bf16 v[188:191], a[128:131], v[124:127], v[188:191]\n"
        "ds_read_b128 v[116:119], v22 offset:50816\n"
        "buffer_load_dwordx4 v13, s[8:11], s93 offen lds\n"
        "v_mfma_f32_16x16x32_bf16 v[192:195], a[132:135], v[124:127], v[192:195]\n"
        "v_mfma_f32_16x16x32_bf16 v[196:199], a[128:131], v[128:131], v[196:199]\n"
        "ds_read_b128 v[120:123], v22 offset:51328\n"
        "s_add_u32 m0, s91, 0x14a0\n"
        "v_mfma_f32_16x16x32_bf16 v[200:203], a[132:135], v[128:131], v[200:203]\n"
        "s_addk_i32 s93, 4096\n"
        "buffer_load_dwordx4 v13, s[8:11], s93 offen lds\n"
        "s_waitcnt vmcnt(19) lgkmcnt(6)\n"
        "v_mfma_f32_16x16x32_bf16 v[156:159], a[136:139], v[132:135], v[156:159]\n"
        "ds_read_b128 v[124:127], v22 offset:33984\n"
        "v_mfma_f32_16x16x32_bf16 v[160:163], a[140:143], v[132:135], v[160:163]\n"
        "s_add_u32 m0, s91, 0x18c0\n"
        "s_addk_i32 s93, 4096\n"
        "v_mfma_f32_16x16x32_bf16 v[164:167], a[136:139], v[136:139], v[164:167]\n"
        "ds_read_b128 v[128:131], v22 offset:34496\n"
        "buffer_load_dwordx4 v13, s[8:11], s93 offen lds\n"
        "v_mfma_f32_16x16x32_bf16 v[168:171], a[140:143], v[136:139], v[168:171]\n"
        "s_waitcnt lgkmcnt(6)\n"
        "v_mfma_f32_16x16x32_bf16 v[188:191], a[136:139], v[140:143], v[188:191]\n"
        "ds_read_b128 v[132:135], v22 offset:50880\n"
        "v_mfma_f32_16x16x32_bf16 v[192:195], a[140:143], v[140:143], v[192:195]\n"
        "s_add_u32 m0, s91, 0x1ce0\n"
        "s_addk_i32 s93, 4096\n"
        "v_mfma_f32_16x16x32_bf16 v[196:199], a[136:139], v[144:147], v[196:199]\n"
        "ds_read_b128 v[136:139], v22 offset:51392\n"
        "buffer_load_dwordx4 v13, s[8:11], s93 offen lds\n"
        "v_mfma_f32_16x16x32_bf16 v[200:203], a[140:143], v[144:147], v[200:203]\n"
        "s_waitcnt vmcnt(18) lgkmcnt(6)\n"
        "v_mfma_f32_16x16x32_bf16 v[156:159], a[144:147], v[148:151], v[156:159]\n"
        "ds_read_b128 v[140:143], v22 offset:34048\n"
        "s_add_u32 s33, s90, 0x3\n"
        "v_mfma_f32_16x16x32_bf16 v[160:163], a[148:151], v[148:151], v[160:163]\n"
        "s_and_b32 s33, s33, 3\n"
        "s_mul_i32 s33, s33, 0x8000\n"
        "v_mfma_f32_16x16x32_bf16 v[164:167], a[144:147], v[152:155], v[164:167]\n"
        "ds_read_b128 v[144:147], v22 offset:34560\n"
        "s_add_u32 s33, s33, s31\n"
        "v_mfma_f32_16x16x32_bf16 v[168:171], a[148:151], v[152:155], v[168:171]\n"
        "buffer_load_dwordx4 a[192:195], v5, s[40:43], s33 offen sc1\n"
        "buffer_load_dwordx4 a[196:199], v5, s[40:43], s33 offen offset:2048 sc1\n"
        "s_waitcnt lgkmcnt(6)\n"
        "v_mfma_f32_16x16x32_bf16 v[188:191], a[144:147], v[116:119], v[188:191]\n"
        "ds_read_b128 v[148:151], v22 offset:50944\n"
        "v_mfma_f32_16x16x32_bf16 v[192:195], a[148:151], v[116:119], v[192:195]\n"
        "v_mfma_f32_16x16x32_bf16 v[196:199], a[144:147], v[120:123], v[196:199]\n"
        "ds_read_b128 v[152:155], v22 offset:51456\n"
        "s_addk_i32 s33, 4096\n"
        "v_mfma_f32_16x16x32_bf16 v[200:203], a[148:151], v[120:123], v[200:203]\n"
        "buffer_load_dwordx4 a[200:203], v5, s[40:43], s33 offen sc1\n"
        "s_waitcnt vmcnt(19) lgkmcnt(6)\n"
        "v_mfma_f32_16x16x32_bf16 v[156:159], a[152:155], v[124:127], v[156:159]\n"
        "ds_read_b128 v[116:119], v22 offset:34112\n"
        "v_mfma_f32_16x16x32_bf16 v[160:163], a[156:159], v[124:127], v[160:163]\n"
        "buffer_load_dwordx4 a[204:207], v5, s[40:43], s33 offen offset:2048 sc1\n"
        "v_mfma_f32_16x16x32_bf16 v[164:167], a[152:155], v[128:131], v[164:167]\n"
        "ds_read_b128 v[120:123], v22 offset:34624\n"
        "v_mfma_f32_16x16x32_bf16 v[168:171], a[156:159], v[128:131], v[168:171]\n"
        "s_waitcnt lgkmcnt(6)\n"
        "v_mfma_f32_16x16x32_bf16 v[188:191], a[152:155], v[132:135], v[188:191]\n"
        "ds_read_b128 v[124:127], v22 offset:51008\n"
        "v_mfma_f32_16x16x32_bf16 v[192:195], a[156:159], v[132:135], v[192:195]\n"
        "s_addk_i32 s33, 4096\n"
        "buffer_load_dwordx4 a[208:211], v5, s[40:43], s33 offen sc1\n"
        "v_mfma_f32_16x16x32_bf16 v[196:199], a[152:155], v[136:139], v[196:199]\n"
        "ds_read_b128 v[128:131], v22 offset:51520\n"
        "v_mfma_f32_16x16x32_bf16 v[200:203], a[156:159], v[136:139], v[200:203]\n"
        "s_waitcnt vmcnt(19) lgkmcnt(6)\n"
        "v_mfma_f32_16x16x32_bf16 v[156:159], a[160:163], v[140:143], v[156:159]\n"
        "ds_read_b128 v[132:135], v22 offset:34176\n"
        "buffer_load_dwordx4 a[212:215], v5, s[40:43], s33 offen offset:2048 sc1\n"
        "v_mfma_f32_16x16x32_bf16 v[160:163], a[164:167], v[140:143], v[160:163]\n"
        "v_mfma_f32_16x16x32_bf16 v[164:167], a[160:163], v[144:147], v[164:167]\n"
        "ds_read_b128 v[136:139], v22 offset:34688\n"
        "v_mfma_f32_16x16x32_bf16 v[168:171], a[164:167], v[144:147], v[168:171]\n"
        "s_addk_i32 s33, 4096\n"
        "buffer_load_dwordx4 a[216:219], v5, s[40:43], s33 offen sc1\n"
        "s_waitcnt lgkmcnt(6)\n"
        "v_mfma_f32_16x16x32_bf16 v[188:191], a[160:163], v[148:151], v[188:191]\n"
        "ds_read_b128 v[140:143], v22 offset:51072\n"
        "v_mfma_f32_16x16x32_bf16 v[192:195], a[164:167], v[148:151], v[192:195]\n"
        "v_mfma_f32_16x16x32_bf16 v[196:199], a[160:163], v[152:155], v[196:199]\n"
        "ds_read_b128 v[144:147], v22 offset:51584\n"
        "buffer_load_dwordx4 a[220:223], v5, s[40:43], s33 offen offset:2048 sc1\n"
        "v_mfma_f32_16x16x32_bf16 v[200:203], a[164:167], v[152:155], v[200:203]\n"
        "s_waitcnt vmcnt(20) lgkmcnt(6)\n"
        "v_mfma_f32_16x16x32_bf16 v[156:159], a[168:171], v[116:119], v[156:159]\n"
        "ds_read_b128 v[148:151], v22 offset:34240\n"
        "v_mfma_f32_16x16x32_bf16 v[160:163], a[172:175], v[116:119], v[160:163]\n"
        "s_addk_i32 s33, 4096\n"
        "buffer_load_dwordx4 a[224:227], v5, s[40:43], s33 offen sc1\n"
        "v_mfma_f32_16x16x32_bf16 v[164:167], a[168:171], v[120:123], v[164:167]\n"
        "ds_read_b128 v[152:155], v22 offset:34752\n"
        "v_mfma_f32_16x16x32_bf16 v[168:171], a[172:175], v[120:123], v[168:171]\n"
        "s_waitcnt lgkmcnt(6)\n"
        "v_mfma_f32_16x16x32_bf16 v[188:191], a[168:171], v[124:127], v[188:191]\n"
        "ds_read_b128 v[116:119], v22 offset:51136\n"
        "v_mfma_f32_16x16x32_bf16 v[192:195], a[172:175], v[124:127], v[192:195]\n"
        "buffer_load_dwordx4 a[228:231], v5, s[40:43], s33 offen offset:2048 sc1\n"
        "v_mfma_f32_16x16x32_bf16 v[196:199], a[168:171], v[128:131], v[196:199]\n"
        "ds_read_b128 v[120:123], v22 offset:51648\n"
        "v_mfma_f32_16x16x32_bf16 v[200:203], a[172:175], v[128:131], v[200:203]\n"
        "s_waitcnt vmcnt(20) lgkmcnt(6)\n"
        "v_mfma_f32_16x16x32_bf16 v[156:159], a[176:179], v[132:135], v[156:159]\n"
        "s_addk_i32 s33, 4096\n"
        "buffer_load_dwordx4 a[232:235], v5, s[40:43], s33 offen sc1\n"
        "v_mfma_f32_16x16x32_bf16 v[160:163], a[180:183], v[132:135], v[160:163]\n"
        "s_add_u32 s28, s67, 0x1000\n"
        "buffer_load_dwordx2 v[20:21], v11, s[40:43], s28 offen sc1\n"
        "v_mfma_f32_16x16x32_bf16 v[164:167], a[176:179], v[136:139], v[164:167]\n"
        "v_mfma_f32_16x16x32_bf16 v[168:171], a[180:183], v[136:139], v[168:171]\n"
        "buffer_load_dwordx4 a[236:239], v5, s[40:43], s33 offen offset:2048 sc1\n"
        "s_waitcnt lgkmcnt(4)\n"
        "v_mfma_f32_16x16x32_bf16 v[188:191], a[176:179], v[140:143], v[188:191]\n"
        "v_mfma_f32_16x16x32_bf16 v[192:195], a[180:183], v[140:143], v[192:195]\n"
        "s_waitcnt vmcnt(13) lgkmcnt(0)\n"
        "s_barrier\n"
        "v_mfma_f32_16x16x32_bf16 v[196:199], a[176:179], v[144:147], v[196:199]\n"
        "ds_read_b128 v[124:127], v16\n"
        "s_addk_i32 s33, 4096\n"
        "v_mfma_f32_16x16x32_bf16 v[200:203], a[180:183], v[144:147], v[200:203]\n"
        "ds_read_b128 v[128:131], v16 offset:512\n"
        "buffer_load_dwordx4 a[240:243], v5, s[40:43], s33 offen sc1\n"
        "v_mfma_f32_16x16x32_bf16 v[156:159], a[184:187], v[148:151], v[156:159]\n"
        "ds_read_b128 v[132:135], v16 offset:16896\n"
        "v_mfma_f32_16x16x32_bf16 v[160:163], a[188:191], v[148:151], v[160:163]\n"
        "ds_read_b128 v[136:139], v16 offset:17408\n"
        "buffer_load_dwordx4 a[244:247], v5, s[40:43], s33 offen offset:2048 sc1\n"
        "v_mfma_f32_16x16x32_bf16 v[164:167], a[184:187], v[152:155], v[164:167]\n"
        "ds_read_b128 v[140:143], v16 offset:64\n"
        "v_mfma_f32_16x16x32_bf16 v[168:171], a[188:191], v[152:155], v[168:171]\n"
        "ds_read_b128 v[144:147], v16 offset:576\n"
        "v_mfma_f32_16x16x32_bf16 v[188:191], a[184:187], v[116:119], v[188:191]\n"
        "ds_read_b128 v[148:151], v16 offset:16960\n"
        "v_mfma_f32_16x16x32_bf16 v[192:195], a[188:191], v[116:119], v[192:195]\n"
        "s_addk_i32 s33, 4096\n"
        "buffer_load_dwordx4 a[248:251], v5, s[40:43], s33 offen sc1\n"
        "v_mfma_f32_16x16x32_bf16 v[196:199], a[184:187], v[120:123], v[196:199]\n"
        "ds_read_b128 v[152:155], v16 offset:17472\n"
        "v_mfma_f32_16x16x32_bf16 v[200:203], a[188:191], v[120:123], v[200:203]\n"
        "s_waitcnt vmcnt(14) lgkmcnt(6)\n"
        "v_mfma_f32_16x16x32_bf16 v[156:159], a[192:195], v[124:127], v[156:159]\n"
        "ds_read_b128 v[116:119], v16 offset:128\n"
        "buffer_load_dwordx4 a[252:255], v5, s[40:43], s33 offen offset:2048 sc1\n"
        "v_mfma_f32_16x16x32_bf16 v[160:163], a[196:199], v[124:127], v[160:163]\n"
        "v_mfma_f32_16x16x32_bf16 v[164:167], a[192:195], v[128:131], v[164:167]\n"
        "ds_read_b128 v[120:123], v16 offset:640\n"
        "v_mfma_f32_16x16x32_bf16 v[168:171], a[196:199], v[128:131], v[168:171]\n"
        "s_waitcnt lgkmcnt(6)\n"
        "v_mfma_f32_16x16x32_bf16 v[188:191], a[192:195], v[132:135], v[188:191]\n"
        "ds_read_b128 v[124:127], v16 offset:17024\n"
        "v_mfma_f32_16x16x32_bf16 v[192:195], a[196:199], v[132:135], v[192:195]\n"
        "s_add_u32 s93, s90, 0x1\n"
        "s_and_b32 s93, s93, 3\n"
        "v_mfma_f32_16x16x32_bf16 v[196:199], a[192:195], v[136:139], v[196:199]\n"
        "ds_read_b128 v[128:131], v16 offset:17536\n"
        "s_lshl_b32 s93, s93, 9\n"
        "v_mfma_f32_16x16x32_bf16 v[200:203], a[196:199], v[136:139], v[200:203]\n"
        "s_add_u32 s93, s93, s92\n"
        "s_add_u32 m0, s91, 0x10800\n"
        "s_waitcnt vmcnt(13) lgkmcnt(6)\n"
        "v_mfma_f32_16x16x32_bf16 v[156:159], a[200:203], v[140:143], v[156:159]\n"
        "ds_read_b128 v[132:135], v16 offset:192\n"
        "buffer_load_dwordx4 v13, s[8:11], s93 offen lds\n"
        "v_mfma_f32_16x16x32_bf16 v[160:163], a[204:207], v[140:143], v[160:163]\n"
        "v_mfma_f32_16x16x32_bf16 v[164:167], a[200:203], v[144:147], v[164:167]\n"
        "ds_read_b128 v[136:139], v16 offset:704\n"
        "s_add_u32 m0, s91, 0x10c20\n"
        "v_mfma_f32_16x16x32_bf16 v[168:171], a[204:207], v[144:147], v[168:171]\n"
        "s_addk_i32 s93, 4096\n"
        "buffer_load_dwordx4 v13, s[8:11], s93 offen lds\n"
        "s_waitcnt lgkmcnt(6)\n"
        "v_mfma_f32_16x16x32_bf16 v[188:191], a[200:203], v[148:151], v[188:191]\n"
        "ds_read_b128 v[140:143], v16 offset:17088\n"
        "v_mfma_f32_16x16x32_bf16 v[192:195], a[204:207], v[148:151], v[192:195]\n"
        "v_mfma_f32_16x16x32_bf16 v[196:199], a[200:203], v[152:155], v[196:199]\n"
        "ds_read_b128 v[144:147], v16 offset:17600\n"
        "v_mfma_f32_16x16x32_bf16 v[200:203], a[204:207], v[152:155], v[200:203]\n"
        "s_add_u32 m0, s91, 0x11040\n"
        "s_addk_i32 s93, 4096\n"
        "s_waitcnt vmcnt(13) lgkmcnt(6)\n"
        "v_mfma_f32_16x16x32_bf16 v[156:159], a[208:211], v[116:119], v[156:159]\n"
        "ds_read_b128 v[148:151], v16 offset:256\n"
        "buffer_load_dwordx4 v13, s[8:11], s93 offen lds\n"
        "v_mfma_f32_16x16x32_bf16 v[160:163], a[212:215], v[116:119], v[160:163]\n"
        "v_mfma_f32_16x16x32_bf16 v[164:167], a[208:211], v[120:123], v[164:167]\n"
        "ds_read_b128 v[152:155], v16 offset:768\n"
        "v_mfma_f32_16x16x32_bf16 v[168:171], a[212:215], v[120:123], v[168:171]\n"
        "s_waitcnt lgkmcnt(6)\n"
        "v_mfma_f32_16x16x32_bf16 v[188:191], a[208:211], v[124:127], v[188:191]\n"
        "ds_read_b128 v[116:119], v16 offset:320\n"
        "v_mfma_f32_16x16x32_bf16 v[192:195], a[212:215], v[124:127], v[192:195]\n"
        "s_add_u32 m0, s91, 0x11460\n"
        "s_addk_i32 s93, 4096\n"
        "v_mfma_f32_16x16x32_bf16 v[196:199], a[208:211], v[128:131], v[196:199]\n"
        "ds_read_b128 v[120:123], v16 offset:832\n"
        "buffer_load_dwordx4 v13, s[8:11], s93 offen lds\n"
        "v_mfma_f32_16x16x32_bf16 v[200:203], a[212:215], v[128:131], v[200:203]\n"
        "s_waitcnt vmcnt(13) lgkmcnt(6)\n"
        "v_mfma_f32_16x16x32_bf16 v[156:159], a[216:219], v[132:135], v[156:159]\n"
        "ds_read_b128 v[124:127], v16 offset:384\n"
        "s_waitcnt vmcnt(9)\n"
        "v_mfma_f32_16x16x32_bf16 v[160:163], a[220:223], v[132:135], v[160:163]\n"
        "s_branch .Lku1\n"
        ".Lkt1:\n"
        "s_setpc_b64 s[86:87]\n"
        ".Lku1:\n"
        "v_cmp_ne_u64 vcc, s[56:57], v[20:21]\n"
        "s_nop 0\n"
        "s_cbranch_vccnz .Lkp1\n"
        ".Lkq1:\n"
        "v_mfma_f32_16x16x32_bf16 v[164:167], a[216:219], v[136:139], v[164:167]\n"
        "ds_read_b128 v[128:131], v16 offset:896\n"
        "s_add_u32 m0, s91, 0x11880\n"
        "v_mfma_f32_16x16x32_bf16 v[168:171], a[220:223], v[136:139], v[168:171]\n"
        "s_addk_i32 s93, 4096\n"
        "buffer_load_dwordx4 v13, s[8:11], s93 offen lds\n"
        "s_waitcnt lgkmcnt(6)\n"
        "v_mfma_f32_16x16x32_bf16 v[188:191], a[216:219], v[140:143], v[188:191]\n"
        "ds_read_b128 v[132:135], v16 offset:448\n"
        "v_mfma_f32_16x16x32_bf16 v[192:195], a[220:223], v[140:143], v[192:195]\n"
        "v_mfma_f32_16x16x32_bf16 v[196:199], a[216:219], v[144:147], v[196:199]\n"
        "ds_read_b128 v[136:139], v16 offset:960\n"
        "v_mfma_f32_16x16x32_bf16 v[200:203], a[220:223], v[144:147], v[200:203]\n"
        "s_add_u32 m0, s91, 0x11ca0\n"
        "s_addk_i32 s93, 4096\n"
        "s_waitcnt lgkmcnt(6)\n"
        "v_mfma_f32_16x16x32_bf16 v[156:159], a[224:227], v[148:151], v[156:159]\n"
        "ds_read_b128 v[140:143], v16 offset:17152\n"
        "buffer_load_dwordx4 v13, s[8:11], s93 offen lds\n"
        "v_mfma_f32_16x16x32_bf16 v[160:163], a[228:231], v[148:151], v[160:163]\n"
        "v_mfma_f32_16x16x32_bf16 v[164:167], a[224:227], v[152:155], v[164:167]\n"
        "ds_read_b128 v[144:147], v16 offset:17664\n"
        "v_mfma_f32_16x16x32_bf16 v[168:171], a[228:231], v[152:155], v[168:171]\n"
        "s_waitcnt vmcnt(10) lgkmcnt(6)\n"
        "v_mfma_f32_16x16x32_bf16 v[156:159], a[232:235], v[116:119], v[156:159]\n"
        "ds_read_b128 v[148:151], v16 offset:17216\n"
        "v_mfma_f32_16x16x32_bf16 v[160:163], a[236:239], v[116:119], v[160:163]\n"
        "s_add_u32 m0, s91, 0x120c0\n"
        "s_addk_i32 s93, 4096\n"
        "v_mfma_f32_16x16x32_bf16 v[164:167], a[232:235], v[120:123], v[164:167]\n"
        "ds_read_b128 v[152:155], v16 offset:17728\n"
        "buffer_load_dwordx4 v13, s[8:11], s93 offen lds\n"
        "v_mfma_f32_16x16x32_bf16 v[168:171], a[236:239], v[120:123], v[168:171]\n"
        "s_waitcnt vmcnt(9) lgkmcnt(6)\n"
        "v_mfma_f32_16x16x32_bf16 v[156:159], a[240:243], v[124:127], v[156:159]\n"
        "ds_read_b128 v[116:119], v16 offset:17280\n"
        "v_mfma_f32_16x16x32_bf16 v[160:163], a[244:247], v[124:127], v[160:163]\n"
        "v_mfma_f32_16x16x32_bf16 v[164:167], a[240:243], v[128:131], v[164:167]\n"
        "ds_read_b128 v[120:123], v16 offset:17792\n"
        "s_add_u32 m0, s91, 0x124e0\n"
        "v_mfma_f32_16x16x32_bf16 v[168:171], a[244:247], v[128:131], v[168:171]\n"
        "s_addk_i32 s93, 4096\n"
        "buffer_load_dwordx4 v13, s[8:11], s93 offen lds\n"
        "s_waitcnt vmcnt(8) lgkmcnt(6)\n"
        "v_mfma_f32_16x16x32_bf16 v[156:159], a[248:251], v[132:135], v[156:159]\n"
        "ds_read_b128 v[124:127], v16 offset:17344\n"
        "v_mfma_f32_16x16x32_bf16 v[160:163], a[252:255], v[132:135], v[160:163]\n"
        "v_mfma_f32_16x16x32_bf16 v[164:167], a[248:251], v[136:139], v[164:167]\n"
        "ds_read_b128 v[128:131], v16 offset:17856\n"
        "v_mfma_f32_16x16x32_bf16 v[168:171], a[252:255], v[136:139], v[168:171]\n"
        "v_min_u32 v239, 13, v1\n"
        "v_lshlrev_b32 v239, 7, v239\n"
        "s_waitcnt lgkmcnt(6)\n"
        "v_mfma_f32_16x16x32_bf16 v[188:191], a[224:227], v[140:143], v[188:191]\n"
        "s_mul_i32 s59, s62, 0x700\n"
        "buffer_load_dword v237, v239, s[20:23], s59 offen\n"
        "v_mfma_f32_16x16x32_bf16 v[192:195], a[228:231], v[140:143], v[192:195]\n"
        "s_cmp_lt_u32 s51, 7\n"
        "s_cselect_b32 s22, 0x1c0000, 0\n"
        "v_mfma_f32_16x16x32_bf16 v[196:199], a[224:227], v[144:147], v[196:199]\n"
        "s_cselect_b32 s46, 0x2c00000, 0\n"
        "v_mfma_f32_16x16x32_bf16 v[200:203], a[228:231], v[144:147], v[200:203]\n"
        "v_cvt_pk_bf16_f32 v92, v156, v157\n"
        "v_cvt_pk_bf16_f32 v93, v158, v159\n"
        "s_waitcnt lgkmcnt(4)\n"
        "v_mfma_f32_16x16x32_bf16 v[188:191], a[232:235], v[148:151], v[188:191]\n"
        "v_cvt_pk_bf16_f32 v94, v160, v161\n"
        "v_cvt_pk_bf16_f32 v95, v162, v163\n"
        "v_mfma_f32_16x16x32_bf16 v[192:195], a[236:239], v[148:151], v[192:195]\n"
        "buffer_store_dwordx4 v[92:95], v5, s[40:43], s35 offen sc1\n"
        "s_add_u32 s93, s90, 0x2\n"
        "s_and_b32 s93, s93, 3\n"
        "s_lshl_b32 s93, s93, 9\n"
        "s_add_u32 s93, s93, s92\n"
        "s_add_u32 m0, s91, 0x18c00\n"
        "s_nop 0\n"
        "buffer_load_dwordx4 v13, s[8:11], s93 offen lds\n"
        "v_cvt_pk_bf16_f32 v96, v164, v165\n"
        "v_cvt_pk_bf16_f32 v97, v166, v167\n"
        "v_cvt_pk_bf16_f32 v98, v168, v169\n"
        "v_cvt_pk_bf16_f32 v99, v170, v171\n"
        "buffer_store_dwordx4 v[96:99], v5, s[40:43], s35 offen offset:2048 sc1\n"
        "s_waitcnt lgkmcnt(0)\n"
        "s_barrier\n"
        "v_mfma_f32_16x16x32_bf16 v[196:199], a[232:235], v[152:155], v[196:199]\n"
        "ds_read_b128 v[132:135], v16 offset:33792\n"
        "v_mfma_f32_16x16x32_bf16 v[200:203], a[236:239], v[152:155], v[200:203]\n"
        "ds_read_b128 v[136:139], v16 offset:34304\n"
        "v_mfma_f32_16x16x32_bf16 v[188:191], a[240:243], v[116:119], v[188:191]\n"
        "ds_read_b128 v[140:143], v16 offset:50688\n"
        "v_mfma_f32_16x16x32_bf16 v[192:195], a[244:247], v[116:119], v[192:195]\n"
        "ds_read_b128 v[144:147], v16 offset:51200\n"
        "v_mfma_f32_16x16x32_bf16 v[196:199], a[240:243], v[120:123], v[196:199]\n"
        "ds_read_b128 v[148:151], v16 offset:33856\n"
        "s_add_u32 m0, s91, 0x19020\n"
        "v_mfma_f32_16x16x32_bf16 v[200:203], a[244:247], v[120:123], v[200:203]\n"
        "ds_read_b128 v[152:155], v16 offset:34368\n"
        "s_addk_i32 s93, 4096\n"
        "v_mfma_f32_16x16x32_bf16 v[188:191], a[248:251], v[124:127], v[188:191]\n"
        "ds_read_b128 v[116:119], v16 offset:50752\n"
        "buffer_load_dwordx4 v13, s[8:11], s93 offen lds\n"
        "v_mfma_f32_16x16x32_bf16 v[192:195], a[252:255], v[124:127], v[192:195]\n"
        "v_mfma_f32_16x16x32_bf16 v[196:199], a[248:251], v[128:131], v[196:199]\n"
        "ds_read_b128 v[120:123], v16 offset:51264\n"
        "v_mfma_f32_16x16x32_bf16 v[200:203], a[252:255], v[128:131], v[200:203]\n"
        "s_add_u32 m0, s91, 0x19440\n"
        "s_addk_i32 s93, 4096\n"
        "s_waitcnt lgkmcnt(6)\n"
        "v_mfma_f32_16x16x32_bf16 v[172:175], a[0:3], v[132:135], 0\n"
        "ds_read_b128 v[124:127], v16 offset:33920\n"
        "buffer_load_dwordx4 v13, s[8:11], s93 offen lds\n"
        "v_mfma_f32_16x16x32_bf16 v[176:179], a[4:7], v[132:135], 0\n"
        "v_mfma_f32_16x16x32_bf16 v[180:183], a[0:3], v[136:139], 0\n"
        "ds_read_b128 v[128:131], v16 offset:34432\n"
        "v_mfma_f32_16x16x32_bf16 v[184:187], a[4:7], v[136:139], 0\n"
        "v_cvt_pk_bf16_f32 v100, v188, v189\n"
        "v_cvt_pk_bf16_f32 v101, v190, v191\n"
        "s_waitcnt lgkmcnt(6)\n"
        "v_mfma_f32_16x16x32_bf16 v[204:207], a[0:3], v[140:143], 0\n"
        "ds_read_b128 v[132:135], v16 offset:50816\n"
        "v_cvt_pk_bf16_f32 v102, v192, v193\n"
        "v_mfma_f32_16x16x32_bf16 v[208:211], a[4:7], v[140:143], 0\n"
        "v_cvt_pk_bf16_f32 v103, v194, v195\n"
        "s_addk_i32 s35, 4096\n"
        "v_mfma_f32_16x16x32_bf16 v[212:215], a[0:3], v[144:147], 0\n"
        "ds_read_b128 v[136:139], v16 offset:51328\n"
        "buffer_store_dwordx4 v[100:103], v5, s[40:43], s35 offen sc1\n"
        "v_mfma_f32_16x16x32_bf16 v[216:219], a[4:7], v[144:147], 0\n"
        "s_add_u32 m0, s91, 0x19860\n"
        "s_addk_i32 s93, 4096\n"
        "s_waitcnt lgkmcnt(6)\n"
        "v_mfma_f32_16x16x32_bf16 v[172:175], a[8:11], v[148:151], v[172:175]\n"
        "ds_read_b128 v[140:143], v16 offset:33984\n"
        "buffer_load_dwordx4 v13, s[8:11], s93 offen lds\n"
        "v_mfma_f32_16x16x32_bf16 v[176:179], a[12:15], v[148:151], v[176:179]\n"
        "v_cvt_pk_bf16_f32 v104, v196, v197\n"
        "v_cvt_pk_bf16_f32 v105, v198, v199\n"
        "v_mfma_f32_16x16x32_bf16 v[180:183], a[8:11], v[152:155], v[180:183]\n"
        "ds_read_b128 v[144:147], v16 offset:34496\n"
        "v_cvt_pk_bf16_f32 v106, v200, v201\n"
        "v_mfma_f32_16x16x32_bf16 v[184:187], a[12:15], v[152:155], v[184:187]\n"
        "v_cvt_pk_bf16_f32 v107, v202, v203\n"
        "buffer_store_dwordx4 v[104:107], v5, s[40:43], s35 offen offset:2048 sc1\n"
        "s_waitcnt lgkmcnt(6)\n"
        "v_mfma_f32_16x16x32_bf16 v[204:207], a[8:11], v[116:119], v[204:207]\n"
        "ds_read_b128 v[148:151], v16 offset:50880\n"
        "s_add_u32 m0, s91, 0x19c80\n"
        "v_mfma_f32_16x16x32_bf16 v[208:211], a[12:15], v[116:119], v[208:211]\n"
        "s_addk_i32 s93, 4096\n"
        "buffer_load_dwordx4 v13, s[8:11], s93 offen lds\n"
        "v_mfma_f32_16x16x32_bf16 v[212:215], a[8:11], v[120:123], v[212:215]\n"
        "ds_read_b128 v[152:155], v16 offset:51392\n"
        "buffer_load_dword v84, v14, s[40:43], s61 offen sc1\n"
        "v_mfma_f32_16x16x32_bf16 v[216:219], a[12:15], v[120:123], v[216:219]\n"
        "s_add_u32 m0, s91, 0x1a0a0\n"
        "s_addk_i32 s93, 4096\n"
        "s_waitcnt lgkmcnt(6)\n"
        "v_mfma_f32_16x16x32_bf16 v[172:175], a[16:19], v[124:127], v[172:175]\n"
        "ds_read_b128 v[116:119], v16 offset:34048\n"
        "buffer_load_dwordx4 v13, s[8:11], s93 offen lds\n"
        "v_mfma_f32_16x16x32_bf16 v[176:179], a[20:23], v[124:127], v[176:179]\n"
        "buffer_load_dword v85, v14, s[40:43], s61 offen offset:256 sc1\n"
        "buffer_load_dword v86, v14, s[40:43], s61 offen offset:512 sc1\n"
        "v_mfma_f32_16x16x32_bf16 v[180:183], a[16:19], v[128:131], v[180:183]\n"
        "ds_read_b128 v[120:123], v16 offset:34560\n"
        "buffer_load_dword v87, v14, s[40:43], s61 offen offset:768 sc1\n"
        "v_mfma_f32_16x16x32_bf16 v[184:187], a[20:23], v[128:131], v[184:187]\n"
        "buffer_load_dword v88, v14, s[40:43], s61 offen offset:2048 sc1\n"
        "buffer_load_dword v89, v14, s[40:43], s61 offen offset:2304 sc1\n"
        "s_waitcnt lgkmcnt(6)\n"
        "v_mfma_f32_16x16x32_bf16 v[204:207], a[16:19], v[132:135], v[204:207]\n"
        "ds_read_b128 v[124:127], v16 offset:50944\n"
        "buffer_load_dword v90, v14, s[40:43], s61 offen offset:2560 sc1\n"
        "v_mfma_f32_16x16x32_bf16 v[208:211], a[20:23], v[132:135], v[208:211]\n"
        "s_add_u32 m0, s91, 0x1a4c0\n"
        "s_addk_i32 s93, 4096\n"
        "v_mfma_f32_16x16x32_bf16 v[212:215], a[16:19], v[136:139], v[212:215]\n"
        "ds_read_b128 v[128:131], v16 offset:51456\n"
        "buffer_load_dwordx4 v13, s[8:11], s93 offen lds\n"
        "v_mfma_f32_16x16x32_bf16 v[216:219], a[20:23], v[136:139], v[216:219]\n"
        "buffer_load_dword v91, v14, s[40:43], s61 offen offset:2816 sc1\n"
        "s_waitcnt lgkmcnt(6)\n"
        "v_mfma_f32_16x16x32_bf16 v[172:175], a[24:27], v[140:143], v[172:175]\n"
        "ds_read_b128 v[132:135], v16 offset:34112\n"
        "v_mfma_f32_16x16x32_bf16 v[176:179], a[28:31], v[140:143], v[176:179]\n"
        "v_mfma_f32_16x16x32_bf16 v[180:183], a[24:27], v[144:147], v[180:183]\n"
        "ds_read_b128 v[136:139], v16 offset:34624\n"
        "s_add_u32 m0, s91, 0x1a8e0\n"
        "v_mfma_f32_16x16x32_bf16 v[184:187], a[28:31], v[144:147], v[184:187]\n"
        "s_addk_i32 s93, 4096\n"
        "buffer_load_dwordx4 v13, s[8:11], s93 offen lds\n"
        "s_waitcnt lgkmcnt(6)\n"
        "v_mfma_f32_16x16x32_bf16 v[204:207], a[24:27], v[148:151], v[204:207]\n"
        "ds_read_b128 v[140:143], v16 offset:51008\n"
        "v_mfma_f32_16x16x32_bf16 v[208:211], a[28:31], v[148:151], v[208:211]\n"
        "v_mfma_f32_16x16x32_bf16 v[212:215], a[24:27], v[152:155], v[212:215]\n"
        "ds_read_b128 v[144:147], v16 offset:51520\n"
        "v_mfma_f32_16x16x32_bf16 v[216:219], a[28:31], v[152:155], v[216:219]\n"
        "buffer_load_dwordx4 v[220:223], v23, s[40:43], s72 offen sc1\n"
        "s_addk_i32 s72, 12288\n"
        "s_waitcnt lgkmcnt(6)\n"
        "v_mfma_f32_16x16x32_bf16 v[172:175], a[32:35], v[116:119], v[172:175]\n"
        "ds_read_b128 v[148:151], v16 offset:34176\n"
        "v_mfma_f32_16x16x32_bf16 v[176:179], a[36:39], v[116:119], v[176:179]\n"
        "v_mfma_f32_16x16x32_bf16 v[180:183], a[32:35], v[120:123], v[180:183]\n"
        "ds_read_b128 v[152:155], v16 offset:34688\n"
        "v_mfma_f32_16x16x32_bf16 v[184:187], a[36:39], v[120:123], v[184:187]\n"
        "s_waitcnt lgkmcnt(6)\n"
        "v_mfma_f32_16x16x32_bf16 v[204:207], a[32:35], v[124:127], v[204:207]\n"
        "ds_read_b128 v[116:119], v16 offset:51072\n"
        "v_mfma_f32_16x16x32_bf16 v[208:211], a[36:39], v[124:127], v[208:211]\n"
        "buffer_load_dwordx4 v[224:227], v23, s[40:43], s72 offen sc1\n"
        "s_addk_i32 s72, 12288\n"
        "v_mfma_f32_16x16x32_bf16 v[212:215], a[32:35], v[128:131], v[212:215]\n"
        "ds_read_b128 v[120:123], v16 offset:51584\n"
        "v_mfma_f32_16x16x32_bf16 v[216:219], a[36:39], v[128:131], v[216:219]\n"
        "s_waitcnt lgkmcnt(6)\n"
        "v_mfma_f32_16x16x32_bf16 v[172:175], a[40:43], v[132:135], v[172:175]\n"
        "ds_read_b128 v[124:127], v16 offset:34240\n"
        "v_mfma_f32_16x16x32_bf16 v[176:179], a[44:47], v[132:135], v[176:179]\n"
        "v_mfma_f32_16x16x32_bf16 v[180:183], a[40:43], v[136:139], v[180:183]\n"
        "ds_read_b128 v[128:131], v16 offset:34752\n"
        "buffer_load_dwordx4 v[228:231], v23, s[40:43], s72 offen sc1\n"
        "v_mfma_f32_16x16x32_bf16 v[184:187], a[44:47], v[136:139], v[184:187]\n"
        "s_addk_i32 s72, 12288\n"
        "s_waitcnt lgkmcnt(6)\n"
        "v_mfma_f32_16x16x32_bf16 v[204:207], a[40:43], v[140:143], v[204:207]\n"
        "ds_read_b128 v[132:135], v16 offset:51136\n"
        "v_mfma_f32_16x16x32_bf16 v[208:211], a[44:47], v[140:143], v[208:211]\n"
        "v_mfma_f32_16x16x32_bf16 v[212:215], a[40:43], v[144:147], v[212:215]\n"
        "ds_read_b128 v[136:139], v16 offset:51648\n"
        "v_mfma_f32_16x16x32_bf16 v[216:219], a[44:47], v[144:147], v[216:219]\n"
        "buffer_load_dwordx4 v[232:235], v23, s[40:43], s72 offen sc1\n"
        "s_addk_i32 s72, 12288\n"
        "s_waitcnt lgkmcnt(6)\n"
        "v_mfma_f32_16x16x32_bf16 v[172:175], a[48:51], v[148:151], v[172:175]\n"
        "v_mfma_f32_16x16x32_bf16 v[176:179], a[52:55], v[148:151], v[176:179]\n"
        "v_mfma_f32_16x16x32_bf16 v[180:183], a[48:51], v[152:155], v[180:183]\n"
        "v_mfma_f32_16x16x32_bf16 v[184:187], a[52:55], v[152:155], v[184:187]\n"
        "s_waitcnt lgkmcnt(4)\n"
        "v_mfma_f32_16x16x32_bf16 v[204:207], a[48:51], v[116:119], v[204:207]\n"
        "v_mfma_f32_16x16x32_bf16 v[208:211], a[52:55], v[116:119], v[208:211]\n"
        "v_min_u32 v239, 13, v1\n"
        "v_lshlrev_b32 v239, 7, v239\n"
        "s_mul_i32 s59, s62, 0x700\n"
        "buffer_load_dword v238, v239, s[24:27], s59 offen\n"
        "s_waitcnt vmcnt(26) lgkmcnt(0)\n"
        "s_barrier\n"
        "v_mfma_f32_16x16x32_bf16 v[212:215], a[48:51], v[120:123], v[212:215]\n"
        "ds_read_b128 v[140:143], v22\n"
        "v_mfma_f32_16x16x32_bf16 v[216:219], a[52:55], v[120:123], v[216:219]\n"
        "ds_read_b128 v[144:147], v22 offset:512\n"
        "v_mfma_f32_16x16x32_bf16 v[172:175], a[56:59], v[124:127], v[172:175]\n"
        "ds_read_b128 v[148:151], v22 offset:16896\n"
        "v_mfma_f32_16x16x32_bf16 v[176:179], a[60:63], v[124:127], v[176:179]\n"
        "ds_read_b128 v[152:155], v22 offset:17408\n"
        "v_mfma_f32_16x16x32_bf16 v[180:183], a[56:59], v[128:131], v[180:183]\n"
        "ds_read_b128 v[116:119], v22 offset:64\n"
        "s_add_u32 s93, s90, 0x3\n"
        "v_mfma_f32_16x16x32_bf16 v[184:187], a[60:63], v[128:131], v[184:187]\n"
        "ds_read_b128 v[120:123], v22 offset:576\n"
        "s_and_b32 s93, s93, 3\n"
        "v_mfma_f32_16x16x32_bf16 v[204:207], a[56:59], v[132:135], v[204:207]\n"
        "ds_read_b128 v[124:127], v22 offset:16960\n"
        "s_lshl_b32 s93, s93, 9\n"
        "v_mfma_f32_16x16x32_bf16 v[208:211], a[60:63], v[132:135], v[208:211]\n"
        "s_add_u32 s93, s93, s92\n"
        "s_add_u32 m0, s91, 0x0\n"
        "v_mfma_f32_16x16x32_bf16 v[212:215], a[56:59], v[136:139], v[212:215]\n"
        "ds_read_b128 v[128:131], v22 offset:17472\n"
        "buffer_load_dwordx4 v13, s[8:11], s93 offen lds\n"
        "v_mfma_f32_16x16x32_bf16 v[216:219], a[60:63], v[136:139], v[216:219]\n"
        "s_add_u32 m0, s91, 0x420\n"
        "s_addk_i32 s93, 4096\n"
        "s_waitcnt lgkmcnt(6)\n"
        "v_mfma_f32_16x16x32_bf16 v[172:175], a[64:67], v[140:143], v[172:175]\n"
        "ds_read_b128 v[132:135], v22 offset:128\n"
        "buffer_load_dwordx4 v13, s[8:11], s93 offen lds\n"
        "v_mfma_f32_16x16x32_bf16 v[176:179], a[68:71], v[140:143], v[176:179]\n"
        "v_mfma_f32_16x16x32_bf16 v[180:183], a[64:67], v[144:147], v[180:183]\n"
        "ds_read_b128 v[136:139], v22 offset:640\n"
        "v_mfma_f32_16x16x32_bf16 v[184:187], a[68:71], v[144:147], v[184:187]\n"
        "s_waitcnt lgkmcnt(6)\n"
        "v_mfma_f32_16x16x32_bf16 v[204:207], a[64:67], v[148:151], v[204:207]\n"
        "ds_read_b128 v[140:143], v22 offset:17024\n"
        "v_mfma_f32_16x16x32_bf16 v[208:211], a[68:71], v[148:151], v[208:211]\n"
        "s_add_u32 m0, s91, 0x840\n"
        "s_addk_i32 s93, 4096\n"
        "v_mfma_f32_16x16x32_bf16 v[212:215], a[64:67], v[152:155], v[212:215]\n"
        "ds_read_b128 v[144:147], v22 offset:17536\n"
        "buffer_load_dwordx4 v13, s[8:11], s93 offen lds\n"
        "v_mfma_f32_16x16x32_bf16 v[216:219], a[68:71], v[152:155], v[216:219]\n"
        "s_waitcnt lgkmcnt(6)\n"
        "v_mfma_f32_16x16x32_bf16 v[172:175], a[72:75], v[116:119], v[172:175]\n"
        "ds_read_b128 v[148:151], v22 offset:192\n"
        "v_mfma_f32_16x16x32_bf16 v[176:179], a[76:79], v[116:119], v[176:179]\n"
        "v_mfma_f32_16x16x32_bf16 v[180:183], a[72:75], v[120:123], v[180:183]\n"
        "ds_read_b128 v[152:155], v22 offset:704\n"
        "s_add_u32 m0, s91, 0xc60\n"
        "v_mfma_f32_16x16x32_bf16 v[184:187], a[76:79], v[120:123], v[184:187]\n"
        "s_addk_i32 s93, 4096\n"
        "buffer_load_dwordx4 v13, s[8:11], s93 offen lds\n"
        "s_waitcnt lgkmcnt(6)\n"
        "v_mfma_f32_16x16x32_bf16 v[204:207], a[72:75], v[124:127], v[204:207]\n"
        "ds_read_b128 v[116:119], v22 offset:17088\n"
        "v_mfma_f32_16x16x32_bf16 v[208:211], a[76:79], v[124:127], v[208:211]\n"
        "v_mfma_f32_16x16x32_bf16 v[212:215], a[72:75], v[128:131], v[212:215]\n"
        "ds_read_b128 v[120:123], v22 offset:17600\n"
        "v_mfma_f32_16x16x32_bf16 v[216:219], a[76:79], v[128:131], v[216:219]\n"
        "s_add_u32 m0, s91, 0x1080\n"
        "s_addk_i32 s93, 4096\n"
        "s_waitcnt lgkmcnt(6)\n"
        "v_mfma_f32_16x16x32_bf16 v[172:175], a[80:83], v[132:135], v[172:175]\n"
        "ds_read_b128 v[124:127], v22 offset:256\n"
        "buffer_load_dwordx4 v13, s[8:11], s93 offen lds\n"
        "v_mfma_f32_16x16x32_bf16 v[176:179], a[84:87], v[132:135], v[176:179]\n"
        "s_waitcnt vmcnt(22)\n"
        "buffer_store_dwordx4 v[252:255], v19, s[40:43], s66 offen sc1\n"
        "v_mfma_f32_16x16x32_bf16 v[180:183], a[80:83], v[136:139], v[180:183]\n"
        "ds_read_b128 v[128:131], v22 offset:768\n"
        "s_xor_b32 s31, s66, 0x400\n"
        "v_mfma_f32_16x16x32_bf16 v[184:187], a[84:87], v[136:139], v[184:187]\n"
        "s_waitcnt lgkmcnt(6)\n"
        "v_mfma_f32_16x16x32_bf16 v[204:207], a[80:83], v[140:143], v[204:207]\n"
        "ds_read_b128 v[132:135], v22 offset:17152\n"
        "v_mfma_f32_16x16x32_bf16 v[208:211], a[84:87], v[140:143], v[208:211]\n"
        "s_add_u32 m0, s91, 0x14a0\n"
        "s_addk_i32 s93, 4096\n"
        "v_mfma_f32_16x16x32_bf16 v[212:215], a[80:83], v[144:147], v[212:215]\n"
        "ds_read_b128 v[136:139], v22 offset:17664\n"
        "buffer_load_dwordx4 v13, s[8:11], s93 offen lds\n"
        "v_mfma_f32_16x16x32_bf16 v[216:219], a[84:87], v[144:147], v[216:219]\n"
        "s_waitcnt lgkmcnt(6)\n"
        "v_mfma_f32_16x16x32_bf16 v[172:175], a[88:91], v[148:151], v[172:175]\n"
        "ds_read_b128 v[140:143], v22 offset:320\n"
        "v_mfma_f32_16x16x32_bf16 v[176:179], a[92:95], v[148:151], v[176:179]\n"
        "v_mfma_f32_16x16x32_bf16 v[180:183], a[88:91], v[152:155], v[180:183]\n"
        "ds_read_b128 v[144:147], v22 offset:832\n"
        "s_add_u32 m0, s91, 0x18c0\n"
        "v_mfma_f32_16x16x32_bf16 v[184:187], a[92:95], v[152:155], v[184:187]\n"
        "s_addk_i32 s93, 4096\n"
        "buffer_load_dwordx4 v13, s[8:11], s93 offen lds\n"
        "s_waitcnt lgkmcnt(6)\n"
        "v_mfma_f32_16x16x32_bf16 v[204:207], a[88:91], v[116:119], v[204:207]\n"
        "ds_read_b128 v[148:151], v22 offset:17216\n"
        "v_mfma_f32_16x16x32_bf16 v[208:211], a[92:95], v[116:119], v[208:211]\n"
        "v_mfma_f32_16x16x32_bf16 v[212:215], a[88:91], v[120:123], v[212:215]\n"
        "ds_read_b128 v[152:155], v22 offset:17728\n"
        "v_mfma_f32_16x16x32_bf16 v[216:219], a[92:95], v[120:123], v[216:219]\n"
        "s_add_u32 m0, s91, 0x1ce0\n"
        "s_addk_i32 s93, 4096\n"
        "s_waitcnt lgkmcnt(6)\n"
        "v_mfma_f32_16x16x32_bf16 v[172:175], a[96:99], v[124:127], v[172:175]\n"
        "ds_read_b128 v[116:119], v22 offset:384\n"
        "buffer_load_dwordx4 v13, s[8:11], s93 offen lds\n"
        "v_mfma_f32_16x16x32_bf16 v[176:179], a[100:103], v[124:127], v[176:179]\n"
        "v_mfma_f32_16x16x32_bf16 v[180:183], a[96:99], v[128:131], v[180:183]\n"
        "ds_read_b128 v[120:123], v22 offset:896\n"
        "v_mfma_f32_16x16x32_bf16 v[184:187], a[100:103], v[128:131], v[184:187]\n"
        "s_waitcnt lgkmcnt(6)\n"
        "v_mfma_f32_16x16x32_bf16 v[204:207], a[96:99], v[132:135], v[204:207]\n"
        "ds_read_b128 v[124:127], v22 offset:17280\n"
        "v_mfma_f32_16x16x32_bf16 v[208:211], a[100:103], v[132:135], v[208:211]\n"
        "s_add_u32 s59, s53, 0x1\n"
        "s_and_b32 s70, s59, 1\n"
        "v_mfma_f32_16x16x32_bf16 v[212:215], a[96:99], v[136:139], v[212:215]\n"
        "ds_read_b128 v[128:131], v22 offset:17792\n"
        "s_lshl_b32 s70, s70, 15\n"
        "v_mfma_f32_16x16x32_bf16 v[216:219], a[100:103], v[136:139], v[216:219]\n"
        "s_add_u32 s70, s70, s71\n"
        "s_and_b32 s59, s59, 2\n"
        "s_waitcnt lgkmcnt(6)\n"
        "v_mfma_f32_16x16x32_bf16 v[172:175], a[104:107], v[140:143], v[172:175]\n"
        "ds_read_b128 v[132:135], v22 offset:448\n"
        "s_cselect_b64 s[12:13], s[18:19], s[16:17]\n"
        "v_mfma_f32_16x16x32_bf16 v[176:179], a[108:111], v[140:143], v[176:179]\n"
        "s_and_b32 s13, s13, 0xffff\n"
        "s_add_u32 s77, s53, 0x0\n"
        "v_mfma_f32_16x16x32_bf16 v[180:183], a[104:107], v[144:147], v[180:183]\n"
        "ds_read_b128 v[136:139], v22 offset:960\n"
        "s_and_b32 s77, s77, 7\n"
        "v_mfma_f32_16x16x32_bf16 v[184:187], a[108:111], v[144:147], v[184:187]\n"
        "s_mul_i32 s77, s77, 0x38000\n"
        "s_add_u32 m0, s74, 0x0\n"
        "s_waitcnt lgkmcnt(6)\n"
        "v_mfma_f32_16x16x32_bf16 v[204:207], a[104:107], v[148:151], v[204:207]\n"
        "ds_read_b128 v[140:143], v22 offset:17344\n"
        "s_add_u32 s77, s77, s76\n"
        "v_mfma_f32_16x16x32_bf16 v[208:211], a[108:111], v[148:151], v[208:211]\n"
        "buffer_load_dwordx4 v26, s[20:23], s77 offen lds\n"
        "buffer_load_dwordx4 v27, s[20:23], s77 offen offset:1024 lds\n"
        "v_mfma_f32_16x16x32_bf16 v[212:215], a[104:107], v[152:155], v[212:215]\n"
        "ds_read_b128 v[144:147], v22 offset:17856\n"
        "s_add_u32 m0, s91, 0x8400\n"
        "v_mfma_f32_16x16x32_bf16 v[216:219], a[108:111], v[152:155], v[216:219]\n"
        "buffer_load_dwordx4 v2, s[12:15], s70 offen lds\n"
        "s_add_u32 m0, s91, 0x8820\n"
        "s_waitcnt lgkmcnt(6)\n"
        "v_mfma_f32_16x16x32_bf16 v[172:175], a[112:115], v[116:119], v[172:175]\n"
        "s_addk_i32 s70, 1024\n"
        "buffer_load_dwordx4 v2, s[12:15], s70 offen lds\n"
        "v_mfma_f32_16x16x32_bf16 v[176:179], a[116:119], v[116:119], v[176:179]\n"
        "v_mfma_f32_16x16x32_bf16 v[180:183], a[112:115], v[120:123], v[180:183]\n"
        "v_mfma_f32_16x16x32_bf16 v[184:187], a[116:119], v[120:123], v[184:187]\n"
        "s_waitcnt lgkmcnt(4)\n"
        "v_mfma_f32_16x16x32_bf16 v[204:207], a[112:115], v[124:127], v[204:207]\n"
        "v_mfma_f32_16x16x32_bf16 v[208:211], a[116:119], v[124:127], v[208:211]\n"
        "s_add_u32 m0, s91, 0x8c40\n"
        "s_addk_i32 s70, 1024\n"
        "buffer_load_dwordx4 v2, s[12:15], s70 offen lds\n"
        "s_waitcnt vmcnt(19) lgkmcnt(0)\n"
        "s_barrier\n"
        "v_mfma_f32_16x16x32_bf16 v[212:215], a[112:115], v[128:131], v[212:215]\n"
        "ds_read_b128 v[148:151], v22 offset:33792\n"
        "v_mfma_f32_16x16x32_bf16 v[216:219], a[116:119], v[128:131], v[216:219]\n"
        "ds_read_b128 v[152:155], v22 offset:34304\n"
        "v_mfma_f32_16x16x32_bf16 v[172:175], a[120:123], v[132:135], v[172:175]\n"
        "ds_read_b128 v[116:119], v22 offset:50688\n"
        "v_mfma_f32_16x16x32_bf16 v[176:179], a[124:127], v[132:135], v[176:179]\n"
        "ds_read_b128 v[120:123], v22 offset:51200\n"
        "v_mfma_f32_16x16x32_bf16 v[180:183], a[120:123], v[136:139], v[180:183]\n"
        "ds_read_b128 v[124:127], v22 offset:33856\n"
        "s_add_u32 m0, s91, 0x9060\n"
        "v_mfma_f32_16x16x32_bf16 v[184:187], a[124:127], v[136:139], v[184:187]\n"
        "ds_read_b128 v[128:131], v22 offset:34368\n"
        "s_addk_i32 s70, 1024\n"
        "v_mfma_f32_16x16x32_bf16 v[204:207], a[120:123], v[140:143], v[204:207]\n"
        "ds_read_b128 v[132:135], v22 offset:50752\n"
        "buffer_load_dwordx4 v2, s[12:15], s70 offen lds\n"
        "v_mfma_f32_16x16x32_bf16 v[208:211], a[124:127], v[140:143], v[208:211]\n"
        "v_mfma_f32_16x16x32_bf16 v[212:215], a[120:123], v[144:147], v[212:215]\n"
        "ds_read_b128 v[136:139], v22 offset:51264\n"
        "s_add_u32 m0, s74, 0x800\n"
        "v_mfma_f32_16x16x32_bf16 v[216:219], a[124:127], v[144:147], v[216:219]\n"
        "s_add_u32 s77, s77, 0x3800\n"
        "buffer_load_dwordx4 v26, s[20:23], s77 offen lds\n"
        "s_waitcnt lgkmcnt(6)\n"
        "v_mfma_f32_16x16x32_bf16 v[172:175], a[128:131], v[148:151], v[172:175]\n"
        "ds_read_b128 v[140:143], v22 offset:33920\n"
        "buffer_load_dwordx4 v27, s[20:23], s77 offen offset:1024 lds\n"
        "v_mfma_f32_16x16x32_bf16 v[176:179], a[132:135], v[148:151], v[176:179]\n"
        "s_add_u32 m0, s91, 0x9480\n"
        "s_addk_i32 s70, 5120\n"
        "v_mfma_f32_16x16x32_bf16 v[180:183], a[128:131], v[152:155], v[180:183]\n"
        "ds_read_b128 v[144:147], v22 offset:34432\n"
        "buffer_load_dwordx4 v2, s[12:15], s70 offen lds\n"
        "v_mfma_f32_16x16x32_bf16 v[184:187], a[132:135], v[152:155], v[184:187]\n"
        "s_waitcnt lgkmcnt(6)\n"
        "v_mfma_f32_16x16x32_bf16 v[204:207], a[128:131], v[116:119], v[204:207]\n"
        "ds_read_b128 v[148:151], v22 offset:50816\n"
        "v_mfma_f32_16x16x32_bf16 v[208:211], a[132:135], v[116:119], v[208:211]\n"
        "s_add_u32 m0, s91, 0x98a0\n"
        "s_addk_i32 s70, 1024\n"
        "v_mfma_f32_16x16x32_bf16 v[212:215], a[128:131], v[120:123], v[212:215]\n"
        "ds_read_b128 v[152:155], v22 offset:51328\n"
        "buffer_load_dwordx4 v2, s[12:15], s70 offen lds\n"
        "v_mfma_f32_16x16x32_bf16 v[216:219], a[132:135], v[120:123], v[216:219]\n"
        "s_waitcnt lgkmcnt(6)\n"
        "v_mfma_f32_16x16x32_bf16 v[172:175], a[136:139], v[124:127], v[172:175]\n"
        "ds_read_b128 v[116:119], v22 offset:33984\n"
        "s_waitcnt vmcnt(20)\n"
        "v_mfma_f32_16x16x32_bf16 v[176:179], a[140:143], v[124:127], v[176:179]\n"
        "v_add_f32 v220, v220, v224\n"
        "v_add_f32 v228, v228, v232\n"
        "v_mfma_f32_16x16x32_bf16 v[180:183], a[136:139], v[128:131], v[180:183]\n"
        "ds_read_b128 v[120:123], v22 offset:34496\n"
        "v_add_f32 v220, v24, v220\n"
        "v_mfma_f32_16x16x32_bf16 v[184:187], a[140:143], v[128:131], v[184:187]\n"
        "v_add_f32 v220, v220, v228\n"
        "v_add_f32 v221, v221, v225\n"
        "s_waitcnt lgkmcnt(6)\n"
        "v_mfma_f32_16x16x32_bf16 v[204:207], a[136:139], v[132:135], v[204:207]\n"
        "ds_read_b128 v[124:127], v22 offset:50880\n"
        "v_add_f32 v229, v229, v233\n"
        "v_mfma_f32_16x16x32_bf16 v[208:211], a[140:143], v[132:135], v[208:211]\n"
        "v_add_f32 v221, v24, v221\n"
        "v_add_f32 v221, v221, v229\n"
        "v_mfma_f32_16x16x32_bf16 v[212:215], a[136:139], v[136:139], v[212:215]\n"
        "ds_read_b128 v[128:131], v22 offset:51392\n"
        "v_add_f32 v222, v222, v226\n"
        "v_mfma_f32_16x16x32_bf16 v[216:219], a[140:143], v[136:139], v[216:219]\n"
        "v_add_f32 v230, v230, v234\n"
        "v_add_f32 v222, v24, v222\n"
        "s_waitcnt lgkmcnt(6)\n"
        "v_mfma_f32_16x16x32_bf16 v[172:175], a[144:147], v[140:143], v[172:175]\n"
        "ds_read_b128 v[132:135], v22 offset:34048\n"
        "v_add_f32 v222, v222, v230\n"
        "v_mfma_f32_16x16x32_bf16 v[176:179], a[148:151], v[140:143], v[176:179]\n"
        "v_add_f32 v223, v223, v227\n"
        "v_add_f32 v231, v231, v235\n"
        "v_mfma_f32_16x16x32_bf16 v[180:183], a[144:147], v[144:147], v[180:183]\n"
        "ds_read_b128 v[136:139], v22 offset:34560\n"
        "v_add_f32 v223, v24, v223\n"
        "v_mfma_f32_16x16x32_bf16 v[184:187], a[148:151], v[144:147], v[184:187]\n"
        "v_add_f32 v223, v223, v231\n"
        "s_lshl_b32 s72, s50, 9\n"
        "s_waitcnt lgkmcnt(6)\n"
        "v_mfma_f32_16x16x32_bf16 v[204:207], a[144:147], v[148:151], v[204:207]\n"
        "ds_read_b128 v[140:143], v22 offset:50944\n"
        "s_add_u32 s72, s72, 0x27000\n"
        "v_mfma_f32_16x16x32_bf16 v[208:211], a[148:151], v[148:151], v[208:211]\n"
        "v_and_b32 v23, 0x1f0, v2\n"
        "v_add_u32 v23, s72, v23\n"
        "v_mfma_f32_16x16x32_bf16 v[212:215], a[144:147], v[152:155], v[212:215]\n"
        "ds_read_b128 v[144:147], v22 offset:51456\n"
        "ds_write_b128 v23, v[220:223]\n"
        "v_mfma_f32_16x16x32_bf16 v[216:219], a[148:151], v[152:155], v[216:219]\n"
        "v_cvt_pk_bf16_f32 v224, v220, v221\n"
        "v_cvt_pk_bf16_f32 v225, v222, v223\n"
        "s_waitcnt lgkmcnt(7)\n"
        "v_mfma_f32_16x16x32_bf16 v[172:175], a[152:155], v[116:119], v[172:175]\n"
        "ds_read_b128 v[148:151], v22 offset:34112\n"
        "buffer_store_dwordx2 v[224:225], v25, s[40:43], s73 offen sc1\n"
        "v_mfma_f32_16x16x32_bf16 v[176:179], a[156:159], v[116:119], v[176:179]\n"
        "s_add_u32 m0, s91, 0x9cc0\n"
        "s_addk_i32 s70, 1024\n"
        "v_mfma_f32_16x16x32_bf16 v[180:183], a[152:155], v[120:123], v[180:183]\n"
        "ds_read_b128 v[152:155], v22 offset:34624\n"
        "buffer_load_dwordx4 v2, s[12:15], s70 offen lds\n"
        "v_mfma_f32_16x16x32_bf16 v[184:187], a[156:159], v[120:123], v[184:187]\n"
        "s_add_u32 m0, s91, 0xa0e0\n"
        "s_addk_i32 s70, 1024\n"
        "s_waitcnt lgkmcnt(7)\n"
        "v_mfma_f32_16x16x32_bf16 v[204:207], a[152:155], v[124:127], v[204:207]\n"
        "ds_read_b128 v[116:119], v22 offset:51008\n"
        "buffer_load_dwordx4 v2, s[12:15], s70 offen lds\n"
        "v_mfma_f32_16x16x32_bf16 v[208:211], a[156:159], v[124:127], v[208:211]\n"
        "s_add_u32 s59, s53, 0x2\n"
        "s_and_b32 s70, s59, 1\n"
        "v_mfma_f32_16x16x32_bf16 v[212:215], a[152:155], v[128:131], v[212:215]\n"
        "ds_read_b128 v[120:123], v22 offset:51520\n"
        "s_lshl_b32 s70, s70, 15\n"
        "v_mfma_f32_16x16x32_bf16 v[216:219], a[156:159], v[128:131], v[216:219]\n"
        "s_add_u32 s70, s70, s71\n"
        "s_and_b32 s59, s59, 2\n"
        "s_waitcnt lgkmcnt(7)\n"
        "v_mfma_f32_16x16x32_bf16 v[172:175], a[160:163], v[132:135], v[172:175]\n"
        "ds_read_b128 v[124:127], v22 offset:34176\n"
        "s_cselect_b64 s[12:13], s[18:19], s[16:17]\n"
        "v_mfma_f32_16x16x32_bf16 v[176:179], a[164:167], v[132:135], v[176:179]\n"
        "s_and_b32 s13, s13, 0xffff\n"
        "s_add_u32 m0, s91, 0x10800\n"
        "v_mfma_f32_16x16x32_bf16 v[180:183], a[160:163], v[136:139], v[180:183]\n"
        "ds_read_b128 v[128:131], v22 offset:34688\n"
        "buffer_load_dwordx4 v2, s[12:15], s70 offen lds\n"
        "v_mfma_f32_16x16x32_bf16 v[184:187], a[164:167], v[136:139], v[184:187]\n"
        "s_add_u32 m0, s74, 0x1000\n"
        "s_add_u32 s77, s77, 0x18800\n"
        "s_waitcnt lgkmcnt(7)\n"
        "v_mfma_f32_16x16x32_bf16 v[204:207], a[160:163], v[140:143], v[204:207]\n"
        "ds_read_b128 v[132:135], v22 offset:51072\n"
        "buffer_load_dwordx4 v26, s[20:23], s77 offen lds\n"
        "v_mfma_f32_16x16x32_bf16 v[208:211], a[164:167], v[140:143], v[208:211]\n"
        "buffer_load_dwordx4 v27, s[20:23], s77 offen offset:1024 lds\n"
        "s_add_u32 m0, s91, 0x10c20\n"
        "v_mfma_f32_16x16x32_bf16 v[212:215], a[160:163], v[144:147], v[212:215]\n"
        "ds_read_b128 v[136:139], v22 offset:51584\n"
        "s_addk_i32 s70, 1024\n"
        "v_mfma_f32_16x16x32_bf16 v[216:219], a[164:167], v[144:147], v[216:219]\n"
        "buffer_load_dwordx4 v2, s[12:15], s70 offen lds\n"
        "s_add_u32 s28, s31, 0x0\n"
        "s_waitcnt lgkmcnt(6)\n"
        "v_mfma_f32_16x16x32_bf16 v[172:175], a[168:171], v[148:151], v[172:175]\n"
        "ds_read_b128 v[140:143], v22 offset:34240\n"
        "buffer_load_dwordx2 v[20:21], v15, s[40:43], s28 offen sc1\n"
        "v_mfma_f32_16x16x32_bf16 v[176:179], a[172:175], v[148:151], v[176:179]\n"
        "s_add_u32 m0, s91, 0x11040\n"
        "s_addk_i32 s70, 1024\n"
        "v_mfma_f32_16x16x32_bf16 v[180:183], a[168:171], v[152:155], v[180:183]\n"
        "ds_read_b128 v[144:147], v22 offset:34752\n"
        "buffer_load_dwordx4 v2, s[12:15], s70 offen lds\n"
        "v_mfma_f32_16x16x32_bf16 v[184:187], a[172:175], v[152:155], v[184:187]\n"
        "v_add_f32 v84, v84, v85\n"
        "v_add_f32 v88, v88, v89\n"
        "s_waitcnt lgkmcnt(6)\n"
        "v_mfma_f32_16x16x32_bf16 v[204:207], a[168:171], v[116:119], v[204:207]\n"
        "ds_read_b128 v[148:151], v22 offset:51136\n"
        "v_add_f32 v86, v86, v87\n"
        "v_mfma_f32_16x16x32_bf16 v[208:211], a[172:175], v[116:119], v[208:211]\n"
        "v_add_f32 v90, v90, v91\n"
        "s_branch .Lku2\n"
        ".Lkt2:\n"
        "s_setpc_b64 s[86:87]\n"
        ".Lku2:\n"
        "v_add_f32 v84, v84, v86\n"
        "v_mfma_f32_16x16x32_bf16 v[212:215], a[168:171], v[120:123], v[212:215]\n"
        "ds_read_b128 v[152:155], v22 offset:51648\n"
        "v_add_f32 v88, v88, v90\n"
        "v_mfma_f32_16x16x32_bf16 v[216:219], a[172:175], v[120:123], v[216:219]\n"
        "v_mov_b32 v85, v84\n"
        "v_mov_b32 v89, v88\n"
        "s_waitcnt lgkmcnt(6)\n"
        "v_mfma_f32_16x16x32_bf16 v[172:175], a[176:179], v[124:127], v[172:175]\n"
        "v_permlane32_swap_b32 v84, v85\n"
        "v_permlane32_swap_b32 v88, v89\n"
        "v_mfma_f32_16x16x32_bf16 v[176:179], a[180:183], v[124:127], v[176:179]\n"
        "v_add_f32 v84, v84, v85\n"
        "v_add_f32 v88, v88, v89\n"
        "v_mfma_f32_16x16x32_bf16 v[180:183], a[176:179], v[128:131], v[180:183]\n"
        "v_mov_b32 v85, v84\n"
        "v_mov_b32 v89, v88\n"
        "v_mfma_f32_16x16x32_bf16 v[184:187], a[180:183], v[128:131], v[184:187]\n"
        "v_permlane16_swap_b32 v84, v85\n"
        "v_permlane16_swap_b32 v88, v89\n"
        "s_waitcnt lgkmcnt(4)\n"
        "v_mfma_f32_16x16x32_bf16 v[204:207], a[176:179], v[132:135], v[204:207]\n"
        "s_add_u32 m0, s91, 0x11460\n"
        "s_addk_i32 s70, 1024\n"
        "v_mfma_f32_16x16x32_bf16 v[208:211], a[180:183], v[132:135], v[208:211]\n"
        "buffer_load_dwordx4 v2, s[12:15], s70 offen lds\n"
        "s_add_u32 m0, s91, 0x11880\n"
        "s_addk_i32 s70, 5120\n"
        "buffer_load_dwordx4 v2, s[12:15], s70 offen lds\n"
        "s_waitcnt vmcnt(19)\n"
        "ds_read_b64_tr_b16 a[0:1], v30\n"
        "ds_read_b64_tr_b16 a[4:5], v30 offset:8\n"
        "ds_read_b64_tr_b16 a[8:9], v31\n"
        "ds_read_b64_tr_b16 a[12:13], v31 offset:8\n"
        "s_waitcnt lgkmcnt(0)\n"
        "s_add_u32 m0, s74, 0x0\n"
        "s_add_u32 s77, s77, 0x3800\n"
        "buffer_load_dwordx4 v26, s[20:23], s77 offen lds\n"
        "buffer_load_dwordx4 v27, s[20:23], s77 offen offset:1024 lds\n"
        "s_barrier\n"
        "v_mfma_f32_16x16x32_bf16 v[212:215], a[176:179], v[136:139], v[212:215]\n"
        "ds_read_b128 v[116:119], v16\n"
        "v_mfma_f32_16x16x32_bf16 v[216:219], a[180:183], v[136:139], v[216:219]\n"
        "ds_read_b128 v[120:123], v16 offset:512\n"
        "v_mfma_f32_16x16x32_bf16 v[172:175], a[184:187], v[140:143], v[172:175]\n"
        "ds_read_b128 v[124:127], v16 offset:16896\n"
        "v_mfma_f32_16x16x32_bf16 v[176:179], a[188:191], v[140:143], v[176:179]\n"
        "ds_read_b128 v[128:131], v16 offset:17408\n"
        "s_add_u32 m0, s91, 0x11ca0\n"
        "v_mfma_f32_16x16x32_bf16 v[180:183], a[184:187], v[144:147], v[180:183]\n"
        "ds_read_b128 v[132:135], v16 offset:64\n"
        "s_addk_i32 s70, 1024\n"
        "v_mfma_f32_16x16x32_bf16 v[184:187], a[188:191], v[144:147], v[184:187]\n"
        "ds_read_b128 v[136:139], v16 offset:576\n"
        "buffer_load_dwordx4 v2, s[12:15], s70 offen lds\n"
        "v_mfma_f32_16x16x32_bf16 v[204:207], a[184:187], v[148:151], v[204:207]\n"
        "ds_read_b128 v[140:143], v16 offset:16960\n"
        "v_mfma_f32_16x16x32_bf16 v[208:211], a[188:191], v[148:151], v[208:211]\n"
        "v_mfma_f32_16x16x32_bf16 v[212:215], a[184:187], v[152:155], v[212:215]\n"
        "ds_read_b128 v[144:147], v16 offset:17472\n"
        "v_mfma_f32_16x16x32_bf16 v[216:219], a[188:191], v[152:155], v[216:219]\n"
        "s_add_u32 m0, s91, 0x120c0\n"
        "s_addk_i32 s70, 1024\n"
        "s_waitcnt lgkmcnt(6)\n"
        "v_mfma_f32_16x16x32_bf16 v[172:175], a[192:195], v[116:119], v[172:175]\n"
        "ds_read_b128 v[148:151], v16 offset:128\n"
        "buffer_load_dwordx4 v2, s[12:15], s70 offen lds\n"
        "v_mfma_f32_16x16x32_bf16 v[176:179], a[196:199], v[116:119], v[176:179]\n"
        "s_waitcnt vmcnt(7)\n"
        "v_cmp_ne_u64 vcc, s[56:57], v[20:21]\n"
        "v_mfma_f32_16x16x32_bf16 v[180:183], a[192:195], v[120:123], v[180:183]\n"
        "ds_read_b128 v[152:155], v16 offset:640\n"
        "s_cbranch_vccnz .Lkp2\n"
        ".Lkq2:\n"
        "v_mfma_f32_16x16x32_bf16 v[184:187], a[196:199], v[120:123], v[184:187]\n"
        "s_xor_b32 s35, s35, 0x20000\n"
        "buffer_load_dwordx4 v[100:103], v5, s[40:43], s35 offen sc1\n"
        "s_waitcnt lgkmcnt(6)\n"
        "v_mfma_f32_16x16x32_bf16 v[204:207], a[192:195], v[124:127], v[204:207]\n"
        "ds_read_b128 v[116:119], v16 offset:17024\n"
        "buffer_load_dwordx4 v[104:107], v5, s[40:43], s35 offen offset:2048 sc1\n"
        "v_mfma_f32_16x16x32_bf16 v[208:211], a[196:199], v[124:127], v[208:211]\n"
        "s_addk_i32 s35, -4096\n"
        "buffer_load_dwordx4 v[92:95], v5, s[40:43], s35 offen sc1\n"
        "v_mfma_f32_16x16x32_bf16 v[212:215], a[192:195], v[128:131], v[212:215]\n"
        "ds_read_b128 v[120:123], v16 offset:17536\n"
        "buffer_load_dwordx4 v[96:99], v5, s[40:43], s35 offen offset:2048 sc1\n"
        "v_mfma_f32_16x16x32_bf16 v[216:219], a[196:199], v[128:131], v[216:219]\n"
        "s_add_u32 m0, s91, 0x124e0\n"
        "s_addk_i32 s70, 1024\n"
        "s_waitcnt lgkmcnt(6)\n"
        "v_mfma_f32_16x16x32_bf16 v[172:175], a[200:203], v[132:135], v[172:175]\n"
        "ds_read_b128 v[124:127], v16 offset:192\n"
        "buffer_load_dwordx4 v2, s[12:15], s70 offen lds\n"
        "v_mfma_f32_16x16x32_bf16 v[176:179], a[204:207], v[132:135], v[176:179]\n"
        "ds_read_b64_tr_b16 a[2:3], v30 offset:2048\n"
        "ds_read_b64_tr_b16 a[6:7], v30 offset:2056\n"
        "v_mfma_f32_16x16x32_bf16 v[180:183], a[200:203], v[136:139], v[180:183]\n"
        "ds_read_b128 v[128:131], v16 offset:704\n"
        "ds_read_b64_tr_b16 a[10:11], v31 offset:2048\n"
        "v_mfma_f32_16x16x32_bf16 v[184:187], a[204:207], v[136:139], v[184:187]\n"
        "ds_read_b64_tr_b16 a[14:15], v31 offset:2056\n"
        "s_waitcnt lgkmcnt(0)\n"
        "v_mfma_f32_16x16x32_bf16 v[204:207], a[200:203], v[140:143], v[204:207]\n"
        "ds_read_b128 v[132:135], v16 offset:17088\n"
        "s_add_u32 s77, s53, 0x1\n"
        "v_mfma_f32_16x16x32_bf16 v[208:211], a[204:207], v[140:143], v[208:211]\n"
        "s_and_b32 s77, s77, 7\n"
        "s_mul_i32 s77, s77, 0x38000\n"
        "v_mfma_f32_16x16x32_bf16 v[212:215], a[200:203], v[144:147], v[212:215]\n"
        "ds_read_b128 v[136:139], v16 offset:17600\n"
        "s_add_u32 m0, s74, 0x800\n"
        "v_mfma_f32_16x16x32_bf16 v[216:219], a[204:207], v[144:147], v[216:219]\n"
        "s_add_u32 s77, s77, s76\n"
        "buffer_load_dwordx4 v26, s[20:23], s77 offen lds\n"
        "v_mfma_f32_16x16x32_bf16 v[172:175], a[208:211], v[148:151], v[172:175]\n"
        "ds_read_b128 v[140:143], v16 offset:256\n"
        "buffer_load_dwordx4 v27, s[20:23], s77 offen offset:1024 lds\n"
        "v_mfma_f32_16x16x32_bf16 v[176:179], a[212:215], v[148:151], v[176:179]\n"
        "ds_read_b128 v[220:223], v10\n"
        "ds_read_b128 v[224:227], v10 offset:16\n"
        "v_mfma_f32_16x16x32_bf16 v[180:183], a[208:211], v[152:155], v[180:183]\n"
        "ds_read_b128 v[144:147], v16 offset:768\n"
        "ds_read_b128 v[228:231], v10 offset:512\n"
        "v_mfma_f32_16x16x32_bf16 v[184:187], a[212:215], v[152:155], v[184:187]\n"
        "ds_read_b128 v[232:235], v10 offset:528\n"
        "v_mfma_f32_16x16x32_bf16 v[204:207], a[208:211], v[116:119], v[204:207]\n"
        "ds_read_b128 v[148:151], v16 offset:17152\n"
        "v_mfma_f32_16x16x32_bf16 v[208:211], a[212:215], v[116:119], v[208:211]\n"
        "v_mfma_f32_16x16x32_bf16 v[212:215], a[208:211], v[120:123], v[212:215]\n"
        "ds_read_b128 v[152:155], v16 offset:17664\n"
        "v_mfma_f32_16x16x32_bf16 v[216:219], a[212:215], v[120:123], v[216:219]\n"
        "v_mfma_f32_16x16x32_bf16 v[172:175], a[216:219], v[124:127], v[172:175]\n"
        "ds_read_b128 v[116:119], v16 offset:320\n"
        "s_waitcnt lgkmcnt(3)\n"
        "v_mfma_f32_16x16x32_bf16 v[176:179], a[220:223], v[124:127], v[176:179]\n"
        "v_mul_f32 v156, v220, v84\n"
        "v_mul_f32 v157, v221, v84\n"
        "v_mfma_f32_16x16x32_bf16 v[180:183], a[216:219], v[128:131], v[180:183]\n"
        "ds_read_b128 v[120:123], v16 offset:832\n"
        "v_mul_f32 v158, v222, v84\n"
        "v_mfma_f32_16x16x32_bf16 v[184:187], a[220:223], v[128:131], v[184:187]\n"
        "v_mul_f32 v159, v223, v84\n"
        "v_mul_f32 v160, v224, v84\n"
        "v_mfma_f32_16x16x32_bf16 v[204:207], a[216:219], v[132:135], v[204:207]\n"
        "ds_read_b128 v[124:127], v16 offset:17216\n"
        "v_mul_f32 v161, v225, v84\n"
        "v_mfma_f32_16x16x32_bf16 v[208:211], a[220:223], v[132:135], v[208:211]\n"
        "v_mul_f32 v162, v226, v84\n"
        "v_mul_f32 v163, v227, v84\n"
        "v_mfma_f32_16x16x32_bf16 v[212:215], a[216:219], v[136:139], v[212:215]\n"
        "ds_read_b128 v[128:131], v16 offset:17728\n"
        "v_mul_f32 v164, v220, v85\n"
        "v_mfma_f32_16x16x32_bf16 v[216:219], a[220:223], v[136:139], v[216:219]\n"
        "v_mul_f32 v165, v221, v85\n"
        "v_mul_f32 v166, v222, v85\n"
        "v_mfma_f32_16x16x32_bf16 v[172:175], a[224:227], v[140:143], v[172:175]\n"
        "ds_read_b128 v[132:135], v16 offset:384\n"
        "v_mul_f32 v167, v223, v85\n"
        "v_mfma_f32_16x16x32_bf16 v[176:179], a[228:231], v[140:143], v[176:179]\n"
        "v_mul_f32 v168, v224, v85\n"
        "v_mul_f32 v169, v225, v85\n"
        "v_mfma_f32_16x16x32_bf16 v[180:183], a[224:227], v[144:147], v[180:183]\n"
        "ds_read_b128 v[136:139], v16 offset:896\n"
        "v_mul_f32 v170, v226, v85\n"
        "v_mfma_f32_16x16x32_bf16 v[184:187], a[228:231], v[144:147], v[184:187]\n"
        "v_mul_f32 v171, v227, v85\n"
        "v_mul_f32 v188, v220, v88\n"
        "s_waitcnt lgkmcnt(6)\n"
        "v_mfma_f32_16x16x32_bf16 v[204:207], a[224:227], v[148:151], v[204:207]\n"
        "ds_read_b128 v[140:143], v16 offset:17280\n"
        "v_mul_f32 v189, v221, v88\n"
        "v_mfma_f32_16x16x32_bf16 v[208:211], a[228:231], v[148:151], v[208:211]\n"
        "v_mul_f32 v190, v222, v88\n"
        "v_mul_f32 v191, v223, v88\n"
        "v_mfma_f32_16x16x32_bf16 v[212:215], a[224:227], v[152:155], v[212:215]\n"
        "ds_read_b128 v[144:147], v16 offset:17792\n"
        "v_mul_f32 v192, v224, v88\n"
        "v_mfma_f32_16x16x32_bf16 v[216:219], a[228:231], v[152:155], v[216:219]\n"
        "v_mul_f32 v193, v225, v88\n"
        "v_mul_f32 v194, v226, v88\n"
        "s_waitcnt lgkmcnt(6)\n"
        "v_mfma_f32_16x16x32_bf16 v[172:175], a[232:235], v[116:119], v[172:175]\n"
        "ds_read_b128 v[148:151], v16 offset:448\n"
        "v_mul_f32 v195, v227, v88\n"
        "v_mfma_f32_16x16x32_bf16 v[176:179], a[236:239], v[116:119], v[176:179]\n"
        "v_mul_f32 v196, v220, v89\n"
        "v_mul_f32 v197, v221, v89\n"
        "v_mfma_f32_16x16x32_bf16 v[180:183], a[232:235], v[120:123], v[180:183]\n"
        "ds_read_b128 v[152:155], v16 offset:960\n"
        "v_mul_f32 v198, v222, v89\n"
        "v_mfma_f32_16x16x32_bf16 v[184:187], a[236:239], v[120:123], v[184:187]\n"
        "v_mul_f32 v199, v223, v89\n"
        "v_mul_f32 v200, v224, v89\n"
        "s_waitcnt lgkmcnt(6)\n"
        "v_mfma_f32_16x16x32_bf16 v[204:207], a[232:235], v[124:127], v[204:207]\n"
        "ds_read_b128 v[116:119], v16 offset:17344\n"
        "v_mul_f32 v201, v225, v89\n"
        "v_mfma_f32_16x16x32_bf16 v[208:211], a[236:239], v[124:127], v[208:211]\n"
        "v_mul_f32 v202, v226, v89\n"
        "v_mul_f32 v203, v227, v89\n"
        "v_mfma_f32_16x16x32_bf16 v[212:215], a[232:235], v[128:131], v[212:215]\n"
        "ds_read_b128 v[120:123], v16 offset:17856\n"
        "ds_read_b64_tr_b16 a[16:17], v30 offset:4096\n"
        "v_mfma_f32_16x16x32_bf16 v[216:219], a[236:239], v[128:131], v[216:219]\n"
        "ds_read_b64_tr_b16 a[20:21], v30 offset:4104\n"
        "ds_read_b64_tr_b16 a[24:25], v31 offset:4096\n"
        "s_waitcnt lgkmcnt(9)\n"
        "v_mfma_f32_16x16x32_bf16 v[172:175], a[240:243], v[132:135], v[172:175]\n"
        "ds_read_b64_tr_b16 a[28:29], v31 offset:4104\n"
        "s_waitcnt lgkmcnt(0)\n"
        "v_mfma_f32_16x16x32_bf16 v[176:179], a[244:247], v[132:135], v[176:179]\n"
        "s_add_u32 m0, s74, 0x1000\n"
        "s_add_u32 s77, s77, 0x3800\n"
        "v_mfma_f32_16x16x32_bf16 v[180:183], a[240:243], v[136:139], v[180:183]\n"
        "buffer_load_dwordx4 v26, s[20:23], s77 offen lds\n"
        "buffer_load_dwordx4 v27, s[20:23], s77 offen offset:1024 lds\n"
        "v_mfma_f32_16x16x32_bf16 v[184:187], a[244:247], v[136:139], v[184:187]\n"
        "s_waitcnt vmcnt(11)\n"
        "ds_read_b64_tr_b16 a[18:19], v30\n"
        "v_mfma_f32_16x16x32_bf16 v[204:207], a[240:243], v[140:143], v[204:207]\n"
        "ds_read_b64_tr_b16 a[22:23], v30 offset:8\n"
        "ds_read_b64_tr_b16 a[26:27], v31\n"
        "v_mfma_f32_16x16x32_bf16 v[208:211], a[244:247], v[140:143], v[208:211]\n"
        "ds_read_b64_tr_b16 a[30:31], v31 offset:8\n"
        "s_waitcnt lgkmcnt(0)\n"
        "s_barrier\n"
        "v_mfma_f32_16x16x32_bf16 v[212:215], a[240:243], v[144:147], v[212:215]\n"
        "s_add_u32 m0, s74, 0x0\n"
        "s_add_u32 s77, s77, 0x18800\n"
        "v_mfma_f32_16x16x32_bf16 v[216:219], a[244:247], v[144:147], v[216:219]\n"
        "buffer_load_dwordx4 v26, s[20:23], s77 offen lds\n"
        "buffer_load_dwordx4 v27, s[20:23], s77 offen offset:1024 lds\n"
        "v_mfma_f32_16x16x32_bf16 v[172:175], a[248:251], v[148:151], v[172:175]\n"
        "v_mfma_f32_16x16x32_bf16 v[176:179], a[252:255], v[148:151], v[176:179]\n"
        "v_mfma_f32_16x16x32_bf16 v[180:183], a[248:251], v[152:155], v[180:183]\n"
        "v_mfma_f32_16x16x32_bf16 v[184:187], a[252:255], v[152:155], v[184:187]\n"
        "v_mfma_f32_16x16x32_bf16 v[204:207], a[248:251], v[116:119], v[204:207]\n"
        "v_mfma_f32_16x16x32_bf16 v[208:211], a[252:255], v[116:119], v[208:211]\n"
        "v_mfma_f32_16x16x32_bf16 v[212:215], a[248:251], v[120:123], v[212:215]\n"
        "v_mfma_f32_16x16x32_bf16 v[216:219], a[252:255], v[120:123], v[216:219]\n"
        "s_waitcnt vmcnt(7)\n"
        "v_mfma_f32_16x16x32_bf16 v[172:175], v[242:245], v[92:95], v[172:175]\n"
        "v_mfma_f32_16x16x32_bf16 v[176:179], v[240:243], v[92:95], v[176:179]\n"
        "v_mfma_f32_16x16x32_bf16 v[180:183], v[242:245], v[96:99], v[180:183]\n"
        "v_mfma_f32_16x16x32_bf16 v[184:187], v[240:243], v[96:99], v[184:187]\n"
        "v_mfma_f32_16x16x32_bf16 v[204:207], v[242:245], v[100:103], v[204:207]\n"
        "v_mfma_f32_16x16x32_bf16 v[208:211], v[240:243], v[100:103], v[208:211]\n"
        "v_mfma_f32_16x16x32_bf16 v[212:215], v[242:245], v[104:107], v[212:215]\n"
        "v_mfma_f32_16x16x32_bf16 v[216:219], v[240:243], v[104:107], v[216:219]\n"
        "s_nop 0\n"
        "v_pk_fma_f32 v[156:157], v[172:173], v[228:229], v[156:157]\n"
        "v_pk_fma_f32 v[158:159], v[174:175], v[230:231], v[158:159]\n"
        "v_pk_fma_f32 v[160:161], v[176:177], v[232:233], v[160:161]\n"
        "v_pk_fma_f32 v[162:163], v[178:179], v[234:235], v[162:163]\n"
        "v_cvt_pk_bf16_f32 v116, v156, v157\n"
        "v_cvt_pk_bf16_f32 v117, v158, v159\n"
        "v_cvt_pk_bf16_f32 v118, v160, v161\n"
        "v_cvt_pk_bf16_f32 v119, v162, v163\n"
        "buffer_store_dwordx4 v[116:119], v5, s[40:43], s34 offen sc1\n"
        "v_pk_fma_f32 v[164:165], v[180:181], v[228:229], v[164:165]\n"
        "v_pk_fma_f32 v[166:167], v[182:183], v[230:231], v[166:167]\n"
        "v_pk_fma_f32 v[168:169], v[184:185], v[232:233], v[168:169]\n"
        "v_pk_fma_f32 v[170:171], v[186:187], v[234:235], v[170:171]\n"
        "v_cvt_pk_bf16_f32 v120, v164, v165\n"
        "v_cvt_pk_bf16_f32 v121, v166, v167\n"
        "v_cvt_pk_bf16_f32 v122, v168, v169\n"
        "v_cvt_pk_bf16_f32 v123, v170, v171\n"
        "buffer_store_dwordx4 v[120:123], v5, s[40:43], s34 offen offset:2048 sc1\n"
        "s_add_u32 s34, s34, 0x22000\n"
        "v_pk_fma_f32 v[188:189], v[204:205], v[228:229], v[188:189]\n"
        "v_pk_fma_f32 v[190:191], v[206:207], v[230:231], v[190:191]\n"
        "v_pk_fma_f32 v[192:193], v[208:209], v[232:233], v[192:193]\n"
        "v_pk_fma_f32 v[194:195], v[210:211], v[234:235], v[194:195]\n"
        "v_cvt_pk_bf16_f32 v124, v188, v189\n"
        "v_cvt_pk_bf16_f32 v125, v190, v191\n"
        "v_cvt_pk_bf16_f32 v126, v192, v193\n"
        "v_cvt_pk_bf16_f32 v127, v194, v195\n"
        "buffer_store_dwordx4 v[124:127], v5, s[40:43], s34 offen sc1\n"
        "v_pk_fma_f32 v[196:197], v[212:213], v[228:229], v[196:197]\n"
        "v_pk_fma_f32 v[198:199], v[214:215], v[230:231], v[198:199]\n"
        "v_pk_fma_f32 v[200:201], v[216:217], v[232:233], v[200:201]\n"
        "v_pk_fma_f32 v[202:203], v[218:219], v[234:235], v[202:203]\n"
        "v_cvt_pk_bf16_f32 v128, v196, v197\n"
        "v_cvt_pk_bf16_f32 v129, v198, v199\n"
        "v_cvt_pk_bf16_f32 v130, v200, v201\n"
        "v_cvt_pk_bf16_f32 v131, v202, v203\n"
        "buffer_store_dwordx4 v[128:131], v5, s[40:43], s34 offen offset:2048 sc1\n"
        "s_add_u32 s59, s53, 0x3\n"
        "s_and_b32 s70, s59, 1\n"
        "s_lshl_b32 s70, s70, 15\n"
        "s_add_u32 s70, s70, s71\n"
        "s_and_b32 s59, s59, 2\n"
        "s_cselect_b64 s[12:13], s[18:19], s[16:17]\n"
        "s_and_b32 s13, s13, 0xffff\n"
        "s_add_u32 m0, s91, 0x18c00\n"
        "s_nop 0\n"
        "buffer_load_dwordx4 v2, s[12:15], s70 offen lds\n"
        "s_add_u32 m0, s91, 0x19020\n"
        "s_addk_i32 s70, 1024\n"
        "buffer_load_dwordx4 v2, s[12:15], s70 offen lds\n"
        "s_add_u32 m0, s91, 0x19440\n"
        "s_addk_i32 s70, 1024\n"
        "buffer_load_dwordx4 v2, s[12:15], s70 offen lds\n"
        "s_add_u32 m0, s91, 0x19860\n"
        "s_addk_i32 s70, 1024\n"
        "buffer_load_dwordx4 v2, s[12:15], s70 offen lds\n"
        "s_add_u32 m0, s91, 0x19c80\n"
        "s_addk_i32 s70, 5120\n"
        "buffer_load_dwordx4 v2, s[12:15], s70 offen lds\n"
        "s_add_u32 m0, s91, 0x1a0a0\n"
        "s_addk_i32 s70, 1024\n"
        "buffer_load_dwordx4 v2, s[12:15], s70 offen lds\n"
        "s_add_u32 m0, s91, 0x1a4c0\n"
        "s_addk_i32 s70, 1024\n"
        "buffer_load_dwordx4 v2, s[12:15], s70 offen lds\n"
        "v_lshl_add_u32 v9, v4, 10, v3\n"
        "s_lshl_b32 s55, s52, 1\n"
        "s_add_u32 s55, s55, s29\n"
        "s_lshl_b32 s31, s55, 15\n"
        "s_lshl_b32 s55, s95, 1\n"
        "s_add_u32 s55, s55, s30\n"
        "s_lshl_b32 s58, s55, 6\n"
        "s_add_u32 s31, s31, s58\n"
        "s_add_u32 s33, s31, 0x4000\n"
        "s_lshl_b32 s61, s55, 1\n"
        "s_add_u32 s61, s61, s29\n"
        "s_mul_i32 s61, s61, 0x1000\n"
        "s_mul_i32 s35, s52, 0x22000\n"
        "s_add_u32 s35, s35, 0x700000\n"
        "s_add_u32 s61, s61, s35\n"
        "s_lshl_b32 s62, s52, 1\n"
        "s_add_u32 s62, s62, s29\n"
        "s_lshl_b32 s62, s62, 12\n"
        "s_add_u32 s62, s62, 0xa0000\n"
        "s_lshl_b32 s55, s55, 8\n"
        "s_add_u32 s63, s55, s62\n"
        "s_add_u32 s55, s30, 0x1\n"
        "s_sub_u32 s55, s55, s53\n"
        "s_and_b32 s55, s55, 3\n"
        "s_mul_i32 s55, s55, 0x8400\n"
        "v_add_u32 v22, s55, v16\n"
        "s_mul_i32 s55, s30, 0x3\n"
        "s_sub_u32 s55, s55, s53\n"
        "s_and_b32 s55, s55, 3\n"
        "s_mul_i32 s55, s55, 0x8400\n"
        "v_add_u32 v16, s55, v16\n"
        "s_lshl_b32 s55, s52, 2\n"
        "s_add_u32 s55, s55, s50\n"
        "s_mul_i32 s34, s55, 0x11000\n"
        "s_add_u32 s34, s34, 0xc00000\n"
        "s_lshl_b32 s55, s30, 7\n"
        "s_add_u32 s55, s55, 0x27400\n"
        "v_lshl_add_u32 v10, v3, 1, s55\n"
        "s_sub_u32 s55, s95, s53\n"
        "s_and_b32 s55, s55, 7\n"
        "s_lshl_b32 s55, s55, 1\n"
        "s_add_u32 s55, s55, s30\n"
        "s_lshl_b32 s55, s55, 6\n"
        "s_add_u32 s55, s55, 0x27800\n"
        "v_add_u32 v12, s55, v3\n"
        "v_and_b32 v6, 1, v0\n"
        "v_lshl_add_u32 v6, v6, 10, v3\n"
        "v_add_u32 v13, 0x27800, v6\n"
        "s_lshl_b32 s55, s53, 7\n"
        "v_lshl_add_u32 v15, v0, 3, s55\n"
        "v_and_b32 v15, 0x3ff, v15\n"
        "s_lshl_b32 s73, s52, 1\n"
        "s_add_u32 s73, s73, s30\n"
        "s_lshl_b32 s73, s73, 10\n"
        "s_add_u32 s73, s73, 0x100000\n"
        "s_xor_b32 s55, s50, 2\n"
        "s_mul_i32 s55, s55, 0x4200\n"
        "s_add_u32 s55, s55, 0x10800\n"
        "v_add_u32 v6, s55, v2\n"
        "v_add_u32 v7, s75, v2\n"
        "s_waitcnt vmcnt(7)\n"
        "buffer_store_dwordx4 v[252:255], v19, s[40:43], s66 offen offset:2048 sc1\n"
        "buffer_load_dwordx4 v[244:247], v9, s[4:7], s31 offen\n"
        "buffer_load_dwordx4 v[248:251], v9, s[4:7], s33 offen\n"
        "ds_read_b64_tr_b16 a[32:33], v30 offset:2048\n"
        "ds_read_b64_tr_b16 a[36:37], v30 offset:2056\n"
        "ds_read_b64_tr_b16 a[40:41], v31 offset:2048\n"
        "ds_read_b64_tr_b16 a[44:45], v31 offset:2056\n"
        "ds_read_b64_tr_b16 a[34:35], v30 offset:4096\n"
        "ds_read_b64_tr_b16 a[38:39], v30 offset:4104\n"
        "ds_read_b64_tr_b16 a[42:43], v31 offset:4096\n"
        "ds_read_b64_tr_b16 a[46:47], v31 offset:4104\n"
        "ds_read_b64_tr_b16 a[48:49], v30\n"
        "ds_read_b64_tr_b16 a[52:53], v30 offset:8\n"
        "ds_read_b64_tr_b16 a[56:57], v31\n"
        "ds_read_b64_tr_b16 a[60:61], v31 offset:8\n"
        "s_add_u32 m0, s91, 0x1a8e0\n"
        "s_addk_i32 s70, 1024\n"
        "buffer_load_dwordx4 v2, s[12:15], s70 offen lds\n"
        "s_waitcnt vmcnt(10)\n"
        "s_add_u32 s59, s53, 0x0\n"
        "s_and_b32 s70, s59, 1\n"
        "s_lshl_b32 s70, s70, 15\n"
        "s_add_u32 s70, s70, s71\n"
        "s_and_b32 s59, s59, 2\n"
        "s_cselect_b64 s[12:13], s[18:19], s[16:17]\n"
        "s_and_b32 s13, s13, 0xffff\n"
        "s_add_u32 m0, s91, 0x0\n"
        "s_nop 0\n"
        "buffer_load_dwordx4 v2, s[12:15], s70 offen lds\n"
        "s_waitcnt vmcnt(10)\n"
        "s_add_u32 m0, s91, 0x420\n"
        "s_addk_i32 s70, 1024\n"
        "buffer_load_dwordx4 v2, s[12:15], s70 offen lds\n"
        "s_waitcnt vmcnt(10)\n"
        "s_add_u32 m0, s91, 0x840\n"
        "s_addk_i32 s70, 1024\n"
        "buffer_load_dwordx4 v2, s[12:15], s70 offen lds\n"
        "s_waitcnt vmcnt(10)\n"
        "s_add_u32 m0, s91, 0xc60\n"
        "s_addk_i32 s70, 1024\n"
        "buffer_load_dwordx4 v2, s[12:15], s70 offen lds\n"
        "s_waitcnt vmcnt(10)\n"
        "s_add_u32 m0, s91, 0x1080\n"
        "s_addk_i32 s70, 5120\n"
        "buffer_load_dwordx4 v2, s[12:15], s70 offen lds\n"
        "s_waitcnt vmcnt(10)\n"
        "s_add_u32 m0, s91, 0x14a0\n"
        "s_addk_i32 s70, 1024\n"
        "buffer_load_dwordx4 v2, s[12:15], s70 offen lds\n"
        "s_waitcnt vmcnt(10)\n"
        "s_add_u32 m0, s91, 0x18c0\n"
        "s_addk_i32 s70, 1024\n"
        "buffer_load_dwordx4 v2, s[12:15], s70 offen lds\n"
        "s_waitcnt vmcnt(10)\n"
        "s_add_u32 m0, s91, 0x1ce0\n"
        "s_addk_i32 s70, 1024\n"
        "buffer_load_dwordx4 v2, s[12:15], s70 offen lds\n"
        "s_waitcnt lgkmcnt(8)\n"
        "s_add_u32 m0, s74, 0x800\n"
        "s_add_u32 s77, s77, 0x3800\n"
        "buffer_load_dwordx4 v26, s[20:23], s77 offen lds\n"
        "buffer_load_dwordx4 v27, s[20:23], s77 offen offset:1024 lds\n"
        "s_waitcnt lgkmcnt(4)\n"
        "s_add_u32 s77, s53, 0x2\n"
        "s_and_b32 s77, s77, 7\n"
        "s_mul_i32 s77, s77, 0x38000\n"
        "s_add_u32 m0, s74, 0x1000\n"
        "s_add_u32 s77, s77, s76\n"
        "buffer_load_dwordx4 v26, s[20:23], s77 offen lds\n"
        "buffer_load_dwordx4 v27, s[20:23], s77 offen offset:1024 lds\n"
        "s_waitcnt lgkmcnt(0)\n"
        "s_add_u32 m0, s74, 0x0\n"
        "s_add_u32 s77, s77, 0x3800\n"
        "buffer_load_dwordx4 v26, s[20:23], s77 offen lds\n"
        "buffer_load_dwordx4 v27, s[20:23], s77 offen offset:1024 lds\n"
        "s_waitcnt vmcnt(15)\n"
        "v_lshlrev_b32 v228, 16, v244\n"
        "v_and_b32 v229, 0xffff0000, v244\n"
        "v_lshlrev_b32 v230, 16, v245\n"
        "v_and_b32 v231, 0xffff0000, v245\n"
        "v_lshlrev_b32 v232, 16, v246\n"
        "v_and_b32 v233, 0xffff0000, v246\n"
        "v_lshlrev_b32 v234, 16, v247\n"
        "v_and_b32 v235, 0xffff0000, v247\n"
        "v_lshlrev_b32 v236, 16, v248\n"
        "v_and_b32 v237, 0xffff0000, v248\n"
        "v_lshlrev_b32 v238, 16, v249\n"
        "v_and_b32 v239, 0xffff0000, v249\n"
        "v_lshlrev_b32 v240, 16, v250\n"
        "v_and_b32 v241, 0xffff0000, v250\n"
        "v_lshlrev_b32 v242, 16, v251\n"
        "v_and_b32 v243, 0xffff0000, v251\n"
        "s_waitcnt vmcnt(6)\n"
        "s_add_u32 s28, s65, 0x800\n"
        "buffer_load_dwordx2 v[20:21], v11, s[40:43], s28 offen sc1\n"
        "s_barrier\n"
        "ds_read_b128 v[108:111], v16\n"
        "ds_read_b128 v[112:115], v16 offset:16896\n"
        "ds_read_b128 v[116:119], v22\n"
        "ds_read_b128 v[120:123], v22 offset:16896\n"
        "ds_read_b128 v[124:127], v16 offset:64\n"
        "ds_read_b128 v[128:131], v16 offset:16960\n"
        "ds_read_b128 v[132:135], v22 offset:64\n"
        "ds_read_b128 v[136:139], v22 offset:16960\n"
        "s_waitcnt vmcnt(5)\n"
        "ds_read_b64_tr_b16 a[50:51], v30 offset:2048\n"
        "ds_read_b64_tr_b16 a[54:55], v30 offset:2056\n"
        "ds_read_b64_tr_b16 a[58:59], v31 offset:2048\n"
        "ds_read_b64_tr_b16 a[62:63], v31 offset:2056\n"
        "s_waitcnt vmcnt(3)\n"
        "ds_read_b64_tr_b16 a[64:65], v30 offset:4096\n"
        "ds_read_b64_tr_b16 a[68:69], v30 offset:4104\n"
        "ds_read_b64_tr_b16 a[72:73], v31 offset:4096\n"
        "ds_read_b64_tr_b16 a[76:77], v31 offset:4104\n"
        "s_waitcnt vmcnt(1)\n"
        "ds_read_b64_tr_b16 a[66:67], v30\n"
        "ds_read_b64_tr_b16 a[70:71], v30 offset:8\n"
        "ds_read_b64_tr_b16 a[74:75], v31\n"
        "ds_read_b64_tr_b16 a[78:79], v31 offset:8\n"
        "s_waitcnt vmcnt(0)\n"
        "v_cmp_ne_u64 vcc, s[56:57], v[20:21]\n"
        "s_nop 0\n"
        "s_cbranch_vccnz .Lkp3\n"
        ".Lkq3:\n"
        "s_barrier\n"
        "buffer_load_dwordx4 v[148:151], v5, s[40:43], s34 offen sc1\n"
        "buffer_load_dwordx4 v[152:155], v5, s[40:43], s34 offen offset:2048 sc1\n"
        "s_addk_i32 s34, 4096\n"
        "buffer_load_dwordx4 v[156:159], v5, s[40:43], s34 offen sc1\n"
        "buffer_load_dwordx4 v[160:163], v5, s[40:43], s34 offen offset:2048 sc1\n"
        "s_addk_i32 s34, 4096\n"
        "buffer_load_dwordx4 v[164:167], v5, s[40:43], s34 offen sc1\n"
        "buffer_load_dwordx4 v[168:171], v5, s[40:43], s34 offen offset:2048 sc1\n"
        "s_addk_i32 s34, 4096\n"
        "buffer_load_dwordx4 v[172:175], v5, s[40:43], s34 offen sc1\n"
        "buffer_load_dwordx4 v[176:179], v5, s[40:43], s34 offen offset:2048 sc1\n"
        "s_addk_i32 s34, 4096\n"
        "buffer_load_dwordx2 v[244:245], v15, s[40:43], s73 offen sc1\n"
        "s_waitcnt vmcnt(7) lgkmcnt(15)\n"
        "v_mfma_f32_16x16x32_bf16 v[76:79], v[108:111], v[148:151], 0\n"
        "ds_read_b128 v[140:143], v16 offset:128\n"
        "buffer_load_dwordx4 v[180:183], v5, s[40:43], s34 offen sc1\n"
        "v_mfma_f32_16x16x32_bf16 v[84:87], v[108:111], v[152:155], 0\n"
        "v_mfma_f32_16x16x32_bf16 v[80:83], v[112:115], v[148:151], 0\n"
        "ds_read_b128 v[144:147], v16 offset:17024\n"
        "buffer_load_dwordx4 v[184:187], v5, s[40:43], s34 offen offset:2048 sc1\n"
        "v_mfma_f32_16x16x32_bf16 v[88:91], v[112:115], v[152:155], 0\n"
        "s_addk_i32 s34, 4096\n"
        "v_mfma_f32_16x16x32_bf16 v[92:95], v[116:119], v[148:151], 0\n"
        "ds_read_b128 v[108:111], v22 offset:128\n"
        "buffer_load_dwordx4 v[188:191], v5, s[40:43], s34 offen sc1\n"
        "v_mfma_f32_16x16x32_bf16 v[100:103], v[116:119], v[152:155], 0\n"
        "v_mfma_f32_16x16x32_bf16 v[96:99], v[120:123], v[148:151], 0\n"
        "ds_read_b128 v[112:115], v22 offset:17024\n"
        "v_mfma_f32_16x16x32_bf16 v[104:107], v[120:123], v[152:155], 0\n"
        "s_waitcnt vmcnt(8) lgkmcnt(15)\n"
        "v_mfma_f32_16x16x32_bf16 v[76:79], v[124:127], v[156:159], v[76:79]\n"
        "ds_read_b128 v[116:119], v16 offset:192\n"
        "buffer_load_dwordx4 v[192:195], v5, s[40:43], s34 offen offset:2048 sc1\n"
        "v_mfma_f32_16x16x32_bf16 v[84:87], v[124:127], v[160:163], v[84:87]\n"
        "s_addk_i32 s34, 4096\n"
        "v_mfma_f32_16x16x32_bf16 v[80:83], v[128:131], v[156:159], v[80:83]\n"
        "ds_read_b128 v[120:123], v16 offset:17088\n"
        "v_mfma_f32_16x16x32_bf16 v[88:91], v[128:131], v[160:163], v[88:91]\n"
        "v_mfma_f32_16x16x32_bf16 v[92:95], v[132:135], v[156:159], v[92:95]\n"
        "ds_read_b128 v[124:127], v22 offset:192\n"
        "buffer_load_dwordx4 v[196:199], v5, s[40:43], s34 offen sc1\n"
        "v_mfma_f32_16x16x32_bf16 v[100:103], v[132:135], v[160:163], v[100:103]\n"
        "v_mfma_f32_16x16x32_bf16 v[96:99], v[136:139], v[156:159], v[96:99]\n"
        "ds_read_b128 v[128:131], v22 offset:17088\n"
        "v_mfma_f32_16x16x32_bf16 v[104:107], v[136:139], v[160:163], v[104:107]\n"
        "s_waitcnt vmcnt(8) lgkmcnt(6)\n"
        "v_mfma_f32_16x16x32_bf16 v[76:79], v[140:143], v[164:167], v[76:79]\n"
        "ds_read_b128 v[132:135], v16 offset:256\n"
        "buffer_load_dwordx4 v[200:203], v5, s[40:43], s34 offen offset:2048 sc1\n"
        "v_mfma_f32_16x16x32_bf16 v[84:87], v[140:143], v[168:171], v[84:87]\n"
        "s_addk_i32 s34, 4096\n"
        "v_mfma_f32_16x16x32_bf16 v[80:83], v[144:147], v[164:167], v[80:83]\n"
        "ds_read_b128 v[136:139], v16 offset:17152\n"
        "v_mfma_f32_16x16x32_bf16 v[88:91], v[144:147], v[168:171], v[88:91]\n"
        "s_waitcnt lgkmcnt(6)\n"
        "v_mfma_f32_16x16x32_bf16 v[92:95], v[108:111], v[164:167], v[92:95]\n"
        "ds_read_b128 v[140:143], v22 offset:256\n"
        "buffer_load_dwordx4 v[204:207], v5, s[40:43], s34 offen sc1\n"
        "v_mfma_f32_16x16x32_bf16 v[100:103], v[108:111], v[168:171], v[100:103]\n"
        "v_mfma_f32_16x16x32_bf16 v[96:99], v[112:115], v[164:167], v[96:99]\n"
        "ds_read_b128 v[144:147], v22 offset:17152\n"
        "v_mfma_f32_16x16x32_bf16 v[104:107], v[112:115], v[168:171], v[104:107]\n"
        "s_waitcnt vmcnt(8) lgkmcnt(6)\n"
        "v_mfma_f32_16x16x32_bf16 v[76:79], v[116:119], v[172:175], v[76:79]\n"
        "ds_read_b128 v[108:111], v16 offset:320\n"
        "buffer_load_dwordx4 v[208:211], v5, s[40:43], s34 offen offset:2048 sc1\n"
        "v_mfma_f32_16x16x32_bf16 v[84:87], v[116:119], v[176:179], v[84:87]\n"
        "s_addk_i32 s34, 4096\n"
        "v_mfma_f32_16x16x32_bf16 v[80:83], v[120:123], v[172:175], v[80:83]\n"
        "ds_read_b128 v[112:115], v16 offset:17216\n"
        "v_mfma_f32_16x16x32_bf16 v[88:91], v[120:123], v[176:179], v[88:91]\n"
        "s_waitcnt lgkmcnt(6)\n"
        "v_mfma_f32_16x16x32_bf16 v[92:95], v[124:127], v[172:175], v[92:95]\n"
        "ds_read_b128 v[116:119], v22 offset:320\n"
        "buffer_load_dwordx4 v[212:215], v5, s[40:43], s34 offen sc1\n"
        "v_mfma_f32_16x16x32_bf16 v[100:103], v[124:127], v[176:179], v[100:103]\n"
        "v_mfma_f32_16x16x32_bf16 v[96:99], v[128:131], v[172:175], v[96:99]\n"
        "ds_read_b128 v[120:123], v22 offset:17216\n"
        "v_mfma_f32_16x16x32_bf16 v[104:107], v[128:131], v[176:179], v[104:107]\n"
        "s_waitcnt vmcnt(7) lgkmcnt(6)\n"
        "v_mfma_f32_16x16x32_bf16 v[76:79], v[132:135], v[180:183], v[76:79]\n"
        "ds_read_b128 v[124:127], v16 offset:384\n"
        "buffer_load_dwordx4 v[216:219], v5, s[40:43], s34 offen offset:2048 sc1\n"
        "v_mfma_f32_16x16x32_bf16 v[84:87], v[132:135], v[184:187], v[84:87]\n"
        "s_addk_i32 s34, 4096\n"
        "v_mfma_f32_16x16x32_bf16 v[80:83], v[136:139], v[180:183], v[80:83]\n"
        "ds_read_b128 v[128:131], v16 offset:17280\n"
        "v_mfma_f32_16x16x32_bf16 v[88:91], v[136:139], v[184:187], v[88:91]\n"
        "s_waitcnt lgkmcnt(6)\n"
        "v_mfma_f32_16x16x32_bf16 v[92:95], v[140:143], v[180:183], v[92:95]\n"
        "ds_read_b128 v[132:135], v22 offset:384\n"
        "buffer_load_dwordx4 v[220:223], v5, s[40:43], s34 offen sc1\n"
        "v_mfma_f32_16x16x32_bf16 v[100:103], v[140:143], v[184:187], v[100:103]\n"
        "v_mfma_f32_16x16x32_bf16 v[96:99], v[144:147], v[180:183], v[96:99]\n"
        "ds_read_b128 v[136:139], v22 offset:17280\n"
        "v_mfma_f32_16x16x32_bf16 v[104:107], v[144:147], v[184:187], v[104:107]\n"
        "s_waitcnt vmcnt(7) lgkmcnt(6)\n"
        "v_mfma_f32_16x16x32_bf16 v[76:79], v[108:111], v[188:191], v[76:79]\n"
        "ds_read_b128 v[140:143], v16 offset:448\n"
        "buffer_load_dwordx4 v[224:227], v5, s[40:43], s34 offen offset:2048 sc1\n"
        "v_mfma_f32_16x16x32_bf16 v[84:87], v[108:111], v[192:195], v[84:87]\n"
        "s_addk_i32 s34, 4096\n"
        "v_mfma_f32_16x16x32_bf16 v[80:83], v[112:115], v[188:191], v[80:83]\n"
        "ds_read_b128 v[144:147], v16 offset:17344\n"
        "v_mfma_f32_16x16x32_bf16 v[88:91], v[112:115], v[192:195], v[88:91]\n"
        "s_waitcnt lgkmcnt(6)\n"
        "v_mfma_f32_16x16x32_bf16 v[92:95], v[116:119], v[188:191], v[92:95]\n"
        "ds_read_b128 v[108:111], v22 offset:448\n"
        "buffer_load_dwordx4 v[148:151], v5, s[40:43], s34 offen sc1\n"
        "v_mfma_f32_16x16x32_bf16 v[100:103], v[116:119], v[192:195], v[100:103]\n"
        "v_mfma_f32_16x16x32_bf16 v[96:99], v[120:123], v[188:191], v[96:99]\n"
        "s_branch .Lku3\n"
        ".Lkt3:\n"
        "s_setpc_b64 s[86:87]\n"
        ".Lku3:\n"
        "ds_read_b128 v[112:115], v22 offset:17344\n"
        "v_mfma_f32_16x16x32_bf16 v[104:107], v[120:123], v[192:195], v[104:107]\n"
        "s_waitcnt vmcnt(7) lgkmcnt(6)\n"
        "v_mfma_f32_16x16x32_bf16 v[76:79], v[124:127], v[196:199], v[76:79]\n"
        "ds_read_b128 v[116:119], v16 offset:512\n"
        "buffer_load_dwordx4 v[152:155], v5, s[40:43], s34 offen offset:2048 sc1\n"
        "v_mfma_f32_16x16x32_bf16 v[84:87], v[124:127], v[200:203], v[84:87]\n"
        "s_addk_i32 s34, 4096\n"
        "v_lshlrev_b32 v15, 3, v0\n"
        "v_mfma_f32_16x16x32_bf16 v[80:83], v[128:131], v[196:199], v[80:83]\n"
        "ds_read_b128 v[120:123], v16 offset:17408\n"
        "v_add_u32 v15, 0x27800, v15\n"
        "v_mfma_f32_16x16x32_bf16 v[88:91], v[128:131], v[200:203], v[88:91]\n"
        "ds_write_b64 v15, v[244:245]\n"
        "s_waitcnt lgkmcnt(7)\n"
        "v_mfma_f32_16x16x32_bf16 v[92:95], v[132:135], v[196:199], v[92:95]\n"
        "ds_read_b128 v[124:127], v22 offset:512\n"
        "buffer_load_dwordx4 v[156:159], v5, s[40:43], s34 offen sc1\n"
        "v_mfma_f32_16x16x32_bf16 v[100:103], v[132:135], v[200:203], v[100:103]\n"
        "v_mfma_f32_16x16x32_bf16 v[96:99], v[136:139], v[196:199], v[96:99]\n"
        "ds_read_b128 v[128:131], v22 offset:17408\n"
        "v_mfma_f32_16x16x32_bf16 v[104:107], v[136:139], v[200:203], v[104:107]\n"
        "s_waitcnt vmcnt(7) lgkmcnt(7)\n"
        "v_mfma_f32_16x16x32_bf16 v[76:79], v[140:143], v[204:207], v[76:79]\n"
        "ds_read_b128 v[132:135], v16 offset:576\n"
        "buffer_load_dwordx4 v[160:163], v5, s[40:43], s34 offen offset:2048 sc1\n"
        "v_mfma_f32_16x16x32_bf16 v[84:87], v[140:143], v[208:211], v[84:87]\n"
        "s_addk_i32 s34, 4096\n"
        "v_mfma_f32_16x16x32_bf16 v[80:83], v[144:147], v[204:207], v[80:83]\n"
        "ds_read_b128 v[136:139], v16 offset:17472\n"
        "v_mfma_f32_16x16x32_bf16 v[88:91], v[144:147], v[208:211], v[88:91]\n"
        "s_waitcnt lgkmcnt(7)\n"
        "v_mfma_f32_16x16x32_bf16 v[92:95], v[108:111], v[204:207], v[92:95]\n"
        "ds_read_b128 v[140:143], v22 offset:576\n"
        "buffer_load_dwordx4 v[164:167], v5, s[40:43], s34 offen sc1\n"
        "v_mfma_f32_16x16x32_bf16 v[100:103], v[108:111], v[208:211], v[100:103]\n"
        "v_mfma_f32_16x16x32_bf16 v[96:99], v[112:115], v[204:207], v[96:99]\n"
        "ds_read_b128 v[144:147], v22 offset:17472\n"
        "v_mfma_f32_16x16x32_bf16 v[104:107], v[112:115], v[208:211], v[104:107]\n"
        "s_waitcnt vmcnt(7) lgkmcnt(7)\n"
        "v_mfma_f32_16x16x32_bf16 v[76:79], v[116:119], v[212:215], v[76:79]\n"
        "ds_read_b128 v[108:111], v16 offset:640\n"
        "buffer_load_dwordx4 v[168:171], v5, s[40:43], s34 offen offset:2048 sc1\n"
        "v_mfma_f32_16x16x32_bf16 v[84:87], v[116:119], v[216:219], v[84:87]\n"
        "s_addk_i32 s34, 4096\n"
        "v_mfma_f32_16x16x32_bf16 v[80:83], v[120:123], v[212:215], v[80:83]\n"
        "ds_read_b128 v[112:115], v16 offset:17536\n"
        "v_mfma_f32_16x16x32_bf16 v[88:91], v[120:123], v[216:219], v[88:91]\n"
        "s_waitcnt lgkmcnt(6)\n"
        "v_mfma_f32_16x16x32_bf16 v[92:95], v[124:127], v[212:215], v[92:95]\n"
        "ds_read_b128 v[116:119], v22 offset:640\n"
        "buffer_load_dwordx4 v[172:175], v5, s[40:43], s34 offen sc1\n"
        "v_mfma_f32_16x16x32_bf16 v[100:103], v[124:127], v[216:219], v[100:103]\n"
        "v_mfma_f32_16x16x32_bf16 v[96:99], v[128:131], v[212:215], v[96:99]\n"
        "ds_read_b128 v[120:123], v22 offset:17536\n"
        "v_mfma_f32_16x16x32_bf16 v[104:107], v[128:131], v[216:219], v[104:107]\n"
        "s_waitcnt vmcnt(7) lgkmcnt(6)\n"
        "v_mfma_f32_16x16x32_bf16 v[76:79], v[132:135], v[220:223], v[76:79]\n"
        "ds_read_b128 v[124:127], v16 offset:704\n"
        "buffer_load_dwordx4 v[176:179], v5, s[40:43], s34 offen offset:2048 sc1\n"
        "v_mfma_f32_16x16x32_bf16 v[84:87], v[132:135], v[224:227], v[84:87]\n"
        "s_addk_i32 s34, 4096\n"
        "v_mfma_f32_16x16x32_bf16 v[80:83], v[136:139], v[220:223], v[80:83]\n"
        "ds_read_b128 v[128:131], v16 offset:17600\n"
        "v_mfma_f32_16x16x32_bf16 v[88:91], v[136:139], v[224:227], v[88:91]\n"
        "s_waitcnt lgkmcnt(6)\n"
        "v_mfma_f32_16x16x32_bf16 v[92:95], v[140:143], v[220:223], v[92:95]\n"
        "ds_read_b128 v[132:135], v22 offset:704\n"
        "buffer_load_dwordx4 v[180:183], v5, s[40:43], s34 offen sc1\n"
        "v_mfma_f32_16x16x32_bf16 v[100:103], v[140:143], v[224:227], v[100:103]\n"
        "v_mfma_f32_16x16x32_bf16 v[96:99], v[144:147], v[220:223], v[96:99]\n"
        "ds_read_b128 v[136:139], v22 offset:17600\n"
        "v_mfma_f32_16x16x32_bf16 v[104:107], v[144:147], v[224:227], v[104:107]\n"
        "s_waitcnt vmcnt(7) lgkmcnt(6)\n"
        "v_mfma_f32_16x16x32_bf16 v[76:79], v[108:111], v[148:151], v[76:79]\n"
        "ds_read_b128 v[140:143], v22 offset:768\n"
        "buffer_load_dwordx4 v[184:187], v5, s[40:43], s34 offen offset:2048 sc1\n"
        "v_mfma_f32_16x16x32_bf16 v[84:87], v[108:111], v[152:155], v[84:87]\n"
        "s_addk_i32 s34, 4096\n"
        "v_mfma_f32_16x16x32_bf16 v[80:83], v[112:115], v[148:151], v[80:83]\n"
        "ds_read_b128 v[144:147], v22 offset:17664\n"
        "v_mfma_f32_16x16x32_bf16 v[88:91], v[112:115], v[152:155], v[88:91]\n"
        "s_waitcnt lgkmcnt(6)\n"
        "v_mfma_f32_16x16x32_bf16 v[92:95], v[116:119], v[148:151], v[92:95]\n"
        "ds_read_b128 v[108:111], v22 offset:832\n"
        "buffer_load_dwordx4 v[188:191], v5, s[40:43], s34 offen sc1\n"
        "v_mfma_f32_16x16x32_bf16 v[100:103], v[116:119], v[152:155], v[100:103]\n"
        "v_mfma_f32_16x16x32_bf16 v[96:99], v[120:123], v[148:151], v[96:99]\n"
        "ds_read_b128 v[112:115], v22 offset:17728\n"
        "v_mfma_f32_16x16x32_bf16 v[104:107], v[120:123], v[152:155], v[104:107]\n"
        "s_waitcnt vmcnt(7) lgkmcnt(6)\n"
        "v_mfma_f32_16x16x32_bf16 v[76:79], v[124:127], v[156:159], v[76:79]\n"
        "ds_read_b128 v[116:119], v22 offset:896\n"
        "buffer_load_dwordx4 v[192:195], v5, s[40:43], s34 offen offset:2048 sc1\n"
        "v_mfma_f32_16x16x32_bf16 v[84:87], v[124:127], v[160:163], v[84:87]\n"
        "s_addk_i32 s34, 4096\n"
        "ds_read_b128 v[196:199], v10\n"
        "v_mfma_f32_16x16x32_bf16 v[80:83], v[128:131], v[156:159], v[80:83]\n"
        "ds_read_b128 v[120:123], v22 offset:17792\n"
        "ds_read_b128 v[200:203], v10 offset:16\n"
        "v_mfma_f32_16x16x32_bf16 v[88:91], v[128:131], v[160:163], v[88:91]\n"
        "s_lshl_b32 s55, s30, 31\n"
        "s_waitcnt lgkmcnt(0)\n"
        "v_mfma_f32_16x16x32_bf16 v[92:95], v[132:135], v[156:159], v[92:95]\n"
        "ds_read_b128 v[124:127], v22 offset:960\n"
        "v_xor_b32 v196, s55, v196\n"
        "v_mfma_f32_16x16x32_bf16 v[100:103], v[132:135], v[160:163], v[100:103]\n"
        "v_xor_b32 v197, s55, v197\n"
        "v_xor_b32 v198, s55, v198\n"
        "v_mfma_f32_16x16x32_bf16 v[96:99], v[136:139], v[156:159], v[96:99]\n"
        "ds_read_b128 v[128:131], v22 offset:17856\n"
        "v_xor_b32 v199, s55, v199\n"
        "v_mfma_f32_16x16x32_bf16 v[104:107], v[136:139], v[160:163], v[104:107]\n"
        "v_xor_b32 v200, s55, v200\n"
        "v_xor_b32 v201, s55, v201\n"
        "s_waitcnt vmcnt(6)\n"
        "v_mfma_f32_16x16x32_bf16 v[92:95], v[140:143], v[164:167], v[92:95]\n"
        "ds_read_b128 v[132:135], v16 offset:768\n"
        "v_xor_b32 v202, s55, v202\n"
        "v_mfma_f32_16x16x32_bf16 v[100:103], v[140:143], v[168:171], v[100:103]\n"
        "v_xor_b32 v203, s55, v203\n"
        "s_add_u32 m0, s74, 0x800\n"
        "v_mfma_f32_16x16x32_bf16 v[96:99], v[144:147], v[164:167], v[96:99]\n"
        "ds_read_b128 v[136:139], v16 offset:17664\n"
        "s_add_u32 s77, s77, 0x18800\n"
        "v_mfma_f32_16x16x32_bf16 v[104:107], v[144:147], v[168:171], v[104:107]\n"
        "buffer_load_dwordx4 v26, s[20:23], s77 offen lds\n"
        "s_add_u32 m0, s74, 0x800\n"
        "s_waitcnt vmcnt(5)\n"
        "v_mfma_f32_16x16x32_bf16 v[92:95], v[108:111], v[172:175], v[92:95]\n"
        "ds_read_b128 v[140:143], v16 offset:832\n"
        "buffer_load_dwordx4 v27, s[20:23], s77 offen offset:1024 lds\n"
        "v_mfma_f32_16x16x32_bf16 v[100:103], v[108:111], v[176:179], v[100:103]\n"
        "s_add_u32 m0, s74, 0x1000\n"
        "s_add_u32 s77, s77, 0x3800\n"
        "v_mfma_f32_16x16x32_bf16 v[96:99], v[112:115], v[172:175], v[96:99]\n"
        "ds_read_b128 v[144:147], v16 offset:17728\n"
        "buffer_load_dwordx4 v26, s[20:23], s77 offen lds\n"
        "v_mfma_f32_16x16x32_bf16 v[104:107], v[112:115], v[176:179], v[104:107]\n"
        "s_waitcnt vmcnt(5)\n"
        "v_mfma_f32_16x16x32_bf16 v[92:95], v[116:119], v[180:183], v[92:95]\n"
        "ds_read_b128 v[108:111], v16 offset:896\n"
        "s_add_u32 m0, s74, 0x1000\n"
        "v_mfma_f32_16x16x32_bf16 v[100:103], v[116:119], v[184:187], v[100:103]\n"
        "buffer_load_dwordx4 v27, s[20:23], s77 offen offset:1024 lds\n"
        "v_mfma_f32_16x16x32_bf16 v[96:99], v[120:123], v[180:183], v[96:99]\n"
        "ds_read_b128 v[112:115], v16 offset:17792\n"
        "v_mfma_f32_16x16x32_bf16 v[104:107], v[120:123], v[184:187], v[104:107]\n"
        "s_waitcnt vmcnt(4) lgkmcnt(6)\n"
        "v_mfma_f32_16x16x32_bf16 v[92:95], v[124:127], v[188:191], v[92:95]\n"
        "ds_read_b128 v[116:119], v16 offset:960\n"
        "s_add_u32 s77, s53, 0x3\n"
        "v_mfma_f32_16x16x32_bf16 v[100:103], v[124:127], v[192:195], v[100:103]\n"
        "s_and_b32 s77, s77, 7\n"
        "s_mul_i32 s77, s77, 0x38000\n"
        "v_mfma_f32_16x16x32_bf16 v[96:99], v[128:131], v[188:191], v[96:99]\n"
        "ds_read_b128 v[120:123], v16 offset:17856\n"
        "s_add_u32 m0, s74, 0x0\n"
        "v_mfma_f32_16x16x32_bf16 v[104:107], v[128:131], v[192:195], v[104:107]\n"
        "s_add_u32 s77, s77, s76\n"
        "buffer_load_dwordx4 v26, s[20:23], s77 offen lds\n"
        "s_waitcnt lgkmcnt(6)\n"
        "v_mfma_f32_16x16x32_bf16 v[76:79], v[132:135], v[164:167], v[76:79]\n"
        "s_add_u32 m0, s74, 0x0\n"
        "s_nop 0\n"
        "buffer_load_dwordx4 v27, s[20:23], s77 offen offset:1024 lds\n"
        "v_mfma_f32_16x16x32_bf16 v[84:87], v[132:135], v[168:171], v[84:87]\n"
        "v_mfma_f32_16x16x32_bf16 v[80:83], v[136:139], v[164:167], v[80:83]\n"
        "v_mfma_f32_16x16x32_bf16 v[88:91], v[136:139], v[168:171], v[88:91]\n"
        "s_waitcnt lgkmcnt(0)\n"
        "s_barrier\n"
        "ds_read_b128 v[248:251], v12\n"
        "v_mfma_f32_16x16x32_bf16 v[76:79], v[140:143], v[172:175], v[76:79]\n"
        "ds_write_b128 v6, v[92:95]\n"
        "v_mfma_f32_16x16x32_bf16 v[84:87], v[140:143], v[176:179], v[84:87]\n"
        "v_mfma_f32_16x16x32_bf16 v[80:83], v[144:147], v[172:175], v[80:83]\n"
        "ds_write_b128 v6, v[96:99] offset:1024\n"
        "v_mfma_f32_16x16x32_bf16 v[88:91], v[144:147], v[176:179], v[88:91]\n"
        "v_mfma_f32_16x16x32_bf16 v[76:79], v[108:111], v[180:183], v[76:79]\n"
        "ds_write_b128 v6, v[100:103] offset:2048\n"
        "v_mfma_f32_16x16x32_bf16 v[84:87], v[108:111], v[184:187], v[84:87]\n"
        "v_mfma_f32_16x16x32_bf16 v[80:83], v[112:115], v[180:183], v[80:83]\n"
        "ds_write_b128 v6, v[104:107] offset:3072\n"
        "v_mfma_f32_16x16x32_bf16 v[88:91], v[112:115], v[184:187], v[88:91]\n"
        "v_mfma_f32_16x16x32_bf16 v[76:79], v[116:119], v[188:191], v[76:79]\n"
        "s_add_u32 m0, s75, 0x1000\n"
        "s_add_u32 s77, s77, 0x3800\n"
        "v_mfma_f32_16x16x32_bf16 v[84:87], v[116:119], v[192:195], v[84:87]\n"
        "buffer_load_dwordx4 v26, s[20:23], s77 offen lds\n"
        "buffer_load_dwordx4 v27, s[20:23], s77 offen offset:1024 lds\n"
        "v_mfma_f32_16x16x32_bf16 v[80:83], v[120:123], v[188:191], v[80:83]\n"
        "s_add_u32 m0, s75, 0x1800\n"
        "s_add_u32 s77, s77, 0x18800\n"
        "v_mfma_f32_16x16x32_bf16 v[88:91], v[120:123], v[192:195], v[88:91]\n"
        "buffer_load_dwordx4 v26, s[20:23], s77 offen lds\n"
        "buffer_load_dwordx4 v27, s[20:23], s77 offen offset:1024 lds\n"
        "s_add_u32 m0, s75, 0x2000\n"
        "s_add_u32 s77, s77, 0x3800\n"
        "buffer_load_dwordx4 v26, s[20:23], s77 offen lds\n"
        "buffer_load_dwordx4 v27, s[20:23], s77 offen offset:1024 lds\n"
        "s_waitcnt lgkmcnt(0)\n"
        "s_barrier\n"
        "ds_read_b128 v[148:151], v7\n"
        "ds_read_b128 v[152:155], v7 offset:1024\n"
        "ds_read_b128 v[156:159], v7 offset:2048\n"
        "ds_read_b128 v[160:163], v7 offset:3072\n"
        "v_lshlrev_b32 v204, 16, v248\n"
        "v_and_b32 v205, 0xffff0000, v248\n"
        "v_lshlrev_b32 v206, 16, v249\n"
        "v_and_b32 v207, 0xffff0000, v249\n"
        "v_lshlrev_b32 v208, 16, v250\n"
        "v_and_b32 v209, 0xffff0000, v250\n"
        "v_lshlrev_b32 v210, 16, v251\n"
        "v_and_b32 v211, 0xffff0000, v251\n"
        "s_waitcnt lgkmcnt(3)\n"
        "v_pk_add_f32 v[148:149], v[76:77], v[148:149] neg_lo:[0,1] neg_hi:[0,1]\n"
        "v_pk_fma_f32 v[228:229], v[148:149], v[196:197], v[228:229]\n"
        "v_pk_mul_f32 v[148:149], v[228:229], v[204:205]\n"
        "v_cvt_pk_bf16_f32 v164, v148, v149\n"
        "v_pk_add_f32 v[150:151], v[78:79], v[150:151] neg_lo:[0,1] neg_hi:[0,1]\n"
        "v_pk_fma_f32 v[230:231], v[150:151], v[198:199], v[230:231]\n"
        "v_pk_mul_f32 v[150:151], v[230:231], v[206:207]\n"
        "v_cvt_pk_bf16_f32 v165, v150, v151\n"
        "s_waitcnt lgkmcnt(2)\n"
        "v_pk_add_f32 v[152:153], v[80:81], v[152:153] neg_lo:[0,1] neg_hi:[0,1]\n"
        "v_pk_fma_f32 v[232:233], v[152:153], v[200:201], v[232:233]\n"
        "v_pk_mul_f32 v[152:153], v[232:233], v[208:209]\n"
        "v_cvt_pk_bf16_f32 v166, v152, v153\n"
        "v_pk_add_f32 v[154:155], v[82:83], v[154:155] neg_lo:[0,1] neg_hi:[0,1]\n"
        "v_pk_fma_f32 v[234:235], v[154:155], v[202:203], v[234:235]\n"
        "v_pk_mul_f32 v[154:155], v[234:235], v[210:211]\n"
        "v_cvt_pk_bf16_f32 v167, v154, v155\n"
        "buffer_store_dwordx4 v[164:167], v5, s[40:43], s61 offen sc1\n"
        "s_waitcnt lgkmcnt(1)\n"
        "v_pk_add_f32 v[156:157], v[84:85], v[156:157] neg_lo:[0,1] neg_hi:[0,1]\n"
        "v_pk_fma_f32 v[236:237], v[156:157], v[196:197], v[236:237]\n"
        "v_pk_mul_f32 v[156:157], v[236:237], v[204:205]\n"
        "v_cvt_pk_bf16_f32 v168, v156, v157\n"
        "v_pk_add_f32 v[158:159], v[86:87], v[158:159] neg_lo:[0,1] neg_hi:[0,1]\n"
        "v_pk_fma_f32 v[238:239], v[158:159], v[198:199], v[238:239]\n"
        "v_pk_mul_f32 v[158:159], v[238:239], v[206:207]\n"
        "v_cvt_pk_bf16_f32 v169, v158, v159\n"
        "s_waitcnt lgkmcnt(0)\n"
        "v_pk_add_f32 v[160:161], v[88:89], v[160:161] neg_lo:[0,1] neg_hi:[0,1]\n"
        "v_pk_fma_f32 v[240:241], v[160:161], v[200:201], v[240:241]\n"
        "v_pk_mul_f32 v[160:161], v[240:241], v[208:209]\n"
        "v_cvt_pk_bf16_f32 v170, v160, v161\n"
        "v_pk_add_f32 v[162:163], v[90:91], v[162:163] neg_lo:[0,1] neg_hi:[0,1]\n"
        "v_pk_fma_f32 v[242:243], v[162:163], v[202:203], v[242:243]\n"
        "v_pk_mul_f32 v[162:163], v[242:243], v[210:211]\n"
        "v_cvt_pk_bf16_f32 v171, v162, v163\n"
        "buffer_store_dwordx4 v[168:171], v5, s[40:43], s61 offen offset:2048 sc1\n"
        "v_pk_add_f32 v[172:173], v[228:229], v[230:231]\n"
        "v_pk_mul_f32 v[174:175], v[228:229], v[228:229]\n"
        "v_pk_add_f32 v[172:173], v[172:173], v[232:233]\n"
        "v_pk_fma_f32 v[174:175], v[230:231], v[230:231], v[174:175]\n"
        "v_pk_add_f32 v[172:173], v[172:173], v[234:235]\n"
        "v_pk_fma_f32 v[174:175], v[232:233], v[232:233], v[174:175]\n"
        "v_add_f32 v172, v172, v173\n"
        "v_pk_fma_f32 v[174:175], v[234:235], v[234:235], v[174:175]\n"
        "v_add_f32 v174, v174, v175\n"
        "v_pk_add_f32 v[176:177], v[236:237], v[238:239]\n"
        "v_pk_mul_f32 v[178:179], v[236:237], v[236:237]\n"
        "v_pk_add_f32 v[176:177], v[176:177], v[240:241]\n"
        "v_pk_fma_f32 v[178:179], v[238:239], v[238:239], v[178:179]\n"
        "v_pk_add_f32 v[176:177], v[176:177], v[242:243]\n"
        "v_pk_fma_f32 v[178:179], v[240:241], v[240:241], v[178:179]\n"
        "v_add_f32 v176, v176, v177\n"
        "v_pk_fma_f32 v[178:179], v[242:243], v[242:243], v[178:179]\n"
        "v_add_f32 v178, v178, v179\n"
        "v_permlane32_swap_b32 v172, v174\n"
        "s_nop 0\n"
        "v_permlane32_swap_b32 v176, v178\n"
        "v_add_f32 v172, v172, v174\n"
        "v_add_f32 v176, v176, v178\n"
        "s_nop 1\n"
        "v_permlane16_swap_b32 v172, v176\n"
        "v_add_f32 v172, v172, v176\n"
        "buffer_store_dword v172, v14, s[40:43], s63 offen sc1\n"
        "s_waitcnt vmcnt(13)\n"
        "ds_read_b64_tr_b16 a[80:81], v30 offset:2048\n"
        "ds_read_b64_tr_b16 a[84:85], v30 offset:2056\n"
        "ds_read_b64_tr_b16 a[88:89], v31 offset:2048\n"
        "ds_read_b64_tr_b16 a[92:93], v31 offset:2056\n"
        "ds_read_b128 v[212:215], v13\n"
        "ds_read_b128 v[216:219], v13 offset:64\n"
        "s_waitcnt lgkmcnt(1)\n"
        "v_mfma_f32_16x16x32_bf16 v[44:47], v[212:215], a[0:3], 0\n"
        "v_mfma_f32_16x16x32_bf16 v[48:51], v[212:215], a[4:7], 0\n"
        "v_mfma_f32_16x16x32_bf16 v[52:55], v[212:215], a[8:11], 0\n"
        "v_mfma_f32_16x16x32_bf16 v[56:59], v[212:215], a[12:15], 0\n"
        "ds_read_b128 v[212:215], v13 offset:128\n"
        "s_waitcnt lgkmcnt(1)\n"
        "v_mfma_f32_16x16x32_bf16 v[44:47], v[216:219], a[16:19], v[44:47]\n"
        "v_mfma_f32_16x16x32_bf16 v[48:51], v[216:219], a[20:23], v[48:51]\n"
        "v_mfma_f32_16x16x32_bf16 v[52:55], v[216:219], a[24:27], v[52:55]\n"
        "v_mfma_f32_16x16x32_bf16 v[56:59], v[216:219], a[28:31], v[56:59]\n"
        "s_waitcnt vmcnt(11)\n"
        "ds_read_b64_tr_b16 a[82:83], v30 offset:4096\n"
        "ds_read_b64_tr_b16 a[86:87], v30 offset:4104\n"
        "ds_read_b64_tr_b16 a[90:91], v31 offset:4096\n"
        "ds_read_b64_tr_b16 a[94:95], v31 offset:4104\n"
        "ds_read_b128 v[216:219], v13 offset:192\n"
        "s_waitcnt lgkmcnt(5)\n"
        "v_mfma_f32_16x16x32_bf16 v[44:47], v[212:215], a[32:35], v[44:47]\n"
        "v_mfma_f32_16x16x32_bf16 v[48:51], v[212:215], a[36:39], v[48:51]\n"
        "v_mfma_f32_16x16x32_bf16 v[52:55], v[212:215], a[40:43], v[52:55]\n"
        "v_mfma_f32_16x16x32_bf16 v[56:59], v[212:215], a[44:47], v[56:59]\n"
        "ds_read_b128 v[212:215], v13 offset:256\n"
        "s_waitcnt lgkmcnt(1)\n"
        "v_mfma_f32_16x16x32_bf16 v[44:47], v[216:219], a[48:51], v[44:47]\n"
        "v_mfma_f32_16x16x32_bf16 v[48:51], v[216:219], a[52:55], v[48:51]\n"
        "v_mfma_f32_16x16x32_bf16 v[52:55], v[216:219], a[56:59], v[52:55]\n"
        "v_mfma_f32_16x16x32_bf16 v[56:59], v[216:219], a[60:63], v[56:59]\n"
        "s_add_u32 s77, s53, 0x4\n"
        "s_and_b32 s77, s77, 7\n"
        "s_mul_i32 s77, s77, 0x38000\n"
        "s_add_u32 m0, s75, 0x2800\n"
        "s_add_u32 s77, s77, s76\n"
        "buffer_load_dwordx4 v26, s[20:23], s77 offen lds\n"
        "buffer_load_dwordx4 v27, s[20:23], s77 offen offset:1024 lds\n"
        "s_waitcnt vmcnt(11)\n"
        "ds_read_b64_tr_b16 a[96:97], v30\n"
        "ds_read_b64_tr_b16 a[100:101], v30 offset:8\n"
        "ds_read_b64_tr_b16 a[104:105], v31\n"
        "ds_read_b64_tr_b16 a[108:109], v31 offset:8\n"
        "ds_read_b128 v[216:219], v13 offset:320\n"
        "s_waitcnt lgkmcnt(5)\n"
        "v_mfma_f32_16x16x32_bf16 v[44:47], v[212:215], a[64:67], v[44:47]\n"
        "v_mfma_f32_16x16x32_bf16 v[48:51], v[212:215], a[68:71], v[48:51]\n"
        "v_mfma_f32_16x16x32_bf16 v[52:55], v[212:215], a[72:75], v[52:55]\n"
        "v_mfma_f32_16x16x32_bf16 v[56:59], v[212:215], a[76:79], v[56:59]\n"
        "ds_read_b128 v[212:215], v13 offset:384\n"
        "s_waitcnt lgkmcnt(1)\n"
        "v_mfma_f32_16x16x32_bf16 v[44:47], v[216:219], a[80:83], v[44:47]\n"
        "v_mfma_f32_16x16x32_bf16 v[48:51], v[216:219], a[84:87], v[48:51]\n"
        "v_mfma_f32_16x16x32_bf16 v[52:55], v[216:219], a[88:91], v[52:55]\n"
        "v_mfma_f32_16x16x32_bf16 v[56:59], v[216:219], a[92:95], v[56:59]\n"
        "s_add_u32 m0, s75, 0x3000\n"
        "s_add_u32 s77, s77, 0x3800\n"
        "buffer_load_dwordx4 v26, s[20:23], s77 offen lds\n"
        "buffer_load_dwordx4 v27, s[20:23], s77 offen offset:1024 lds\n"
        "s_waitcnt vmcnt(11)\n"
        "ds_read_b64_tr_b16 a[98:99], v32 offset:4096\n"
        "ds_read_b64_tr_b16 a[102:103], v32 offset:4104\n"
        "ds_read_b64_tr_b16 a[106:107], v33 offset:4096\n"
        "ds_read_b64_tr_b16 a[110:111], v33 offset:4104\n"
        "s_add_u32 m0, s75, 0x3800\n"
        "s_add_u32 s77, s77, 0x18800\n"
        "buffer_load_dwordx4 v26, s[20:23], s77 offen lds\n"
        "buffer_load_dwordx4 v27, s[20:23], s77 offen offset:1024 lds\n"
        "s_waitcnt vmcnt(11)\n"
        "ds_read_b64_tr_b16 a[112:113], v32 offset:6144\n"
        "ds_read_b64_tr_b16 a[116:117], v32 offset:6152\n"
        "ds_read_b64_tr_b16 a[120:121], v33 offset:6144\n"
        "ds_read_b64_tr_b16 a[124:125], v33 offset:6152\n"
        "ds_read_b128 v[216:219], v13 offset:448\n"
        "s_waitcnt lgkmcnt(5)\n"
        "v_mfma_f32_16x16x32_bf16 v[44:47], v[212:215], a[96:99], v[44:47]\n"
        "v_mfma_f32_16x16x32_bf16 v[48:51], v[212:215], a[100:103], v[48:51]\n"
        "v_mfma_f32_16x16x32_bf16 v[52:55], v[212:215], a[104:107], v[52:55]\n"
        "v_mfma_f32_16x16x32_bf16 v[56:59], v[212:215], a[108:111], v[56:59]\n"
        "s_add_u32 m0, s75, 0x0\n"
        "s_add_u32 s77, s77, 0x3800\n"
        "buffer_load_dwordx4 v26, s[20:23], s77 offen lds\n"
        "buffer_load_dwordx4 v27, s[20:23], s77 offen offset:1024 lds\n"
        "s_waitcnt vmcnt(11)\n"
        "ds_read_b64_tr_b16 a[114:115], v32 offset:8192\n"
        "ds_read_b64_tr_b16 a[118:119], v32 offset:8200\n"
        "ds_read_b64_tr_b16 a[122:123], v33 offset:8192\n"
        "ds_read_b64_tr_b16 a[126:127], v33 offset:8200\n"
        "s_add_u32 s77, s53, 0x5\n"
        "s_and_b32 s77, s77, 7\n"
        "s_mul_i32 s77, s77, 0x38000\n"
        "s_add_u32 m0, s75, 0x800\n"
        "s_add_u32 s77, s77, s76\n"
        "buffer_load_dwordx4 v26, s[20:23], s77 offen lds\n"
        "buffer_load_dwordx4 v27, s[20:23], s77 offen offset:1024 lds\n"
        "s_waitcnt vmcnt(10)\n"
        "s_add_u32 s59, s64, 0x1800\n"
        "buffer_store_dwordx4 v[252:255], v19, s[40:43], s59 offen sc1\n"
        "s_waitcnt vmcnt(9)\n"
        "ds_read_b64_tr_b16 a[128:129], v32 offset:10240\n"
        "ds_read_b64_tr_b16 a[132:133], v32 offset:10248\n"
        "ds_read_b64_tr_b16 a[136:137], v33 offset:10240\n"
        "ds_read_b64_tr_b16 a[140:141], v33 offset:10248\n"
        "ds_read_b128 v[212:215], v13 offset:512\n"
        "s_waitcnt lgkmcnt(5)\n"
        "v_mfma_f32_16x16x32_bf16 v[44:47], v[216:219], a[112:115], v[44:47]\n"
        "v_mfma_f32_16x16x32_bf16 v[48:51], v[216:219], a[116:119], v[48:51]\n"
        "v_mfma_f32_16x16x32_bf16 v[52:55], v[216:219], a[120:123], v[52:55]\n"
        "v_mfma_f32_16x16x32_bf16 v[56:59], v[216:219], a[124:127], v[56:59]\n"
        "s_add_u32 m0, s74, 0x800\n"
        "s_add_u32 s77, s77, 0x3800\n"
        "buffer_load_dwordx4 v26, s[20:23], s77 offen lds\n"
        "buffer_load_dwordx4 v27, s[20:23], s77 offen offset:1024 lds\n"
        "s_waitcnt vmcnt(9)\n"
        "ds_read_b64_tr_b16 a[130:131], v32 offset:12288\n"
        "ds_read_b64_tr_b16 a[134:135], v32 offset:12296\n"
        "ds_read_b64_tr_b16 a[138:139], v33 offset:12288\n"
        "ds_read_b64_tr_b16 a[142:143], v33 offset:12296\n"
        "s_add_u32 m0, s74, 0x1000\n"
        "s_add_u32 s77, s77, 0x18800\n"
        "buffer_load_dwordx4 v26, s[20:23], s77 offen lds\n"
        "buffer_load_dwordx4 v27, s[20:23], s77 offen offset:1024 lds\n"
        "s_waitcnt vmcnt(9)\n"
        "ds_read_b64_tr_b16 a[144:145], v32 offset:14336\n"
        "ds_read_b64_tr_b16 a[148:149], v32 offset:14344\n"
        "ds_read_b64_tr_b16 a[152:153], v33 offset:14336\n"
        "ds_read_b64_tr_b16 a[156:157], v33 offset:14344\n"
        "ds_read_b128 v[216:219], v13 offset:576\n"
        "s_waitcnt lgkmcnt(5)\n"
        "v_mfma_f32_16x16x32_bf16 v[44:47], v[212:215], a[128:131], v[44:47]\n"
        "v_mfma_f32_16x16x32_bf16 v[48:51], v[212:215], a[132:135], v[48:51]\n"
        "v_mfma_f32_16x16x32_bf16 v[52:55], v[212:215], a[136:139], v[52:55]\n"
        "v_mfma_f32_16x16x32_bf16 v[56:59], v[212:215], a[140:143], v[56:59]\n"
        "s_add_u32 m0, s74, 0x0\n"
        "s_add_u32 s77, s77, 0x3800\n"
        "buffer_load_dwordx4 v26, s[20:23], s77 offen lds\n"
        "buffer_load_dwordx4 v27, s[20:23], s77 offen offset:1024 lds\n"
        "s_waitcnt vmcnt(9)\n"
        "ds_read_b64_tr_b16 a[146:147], v32\n"
        "ds_read_b64_tr_b16 a[150:151], v32 offset:8\n"
        "ds_read_b64_tr_b16 a[154:155], v33\n"
        "ds_read_b64_tr_b16 a[158:159], v33 offset:8\n"
        "s_add_u32 s77, s53, 0x6\n"
        "s_and_b32 s77, s77, 7\n"
        "s_mul_i32 s77, s77, 0x38000\n"
        "s_add_u32 m0, s75, 0x1000\n"
        "s_add_u32 s77, s77, s76\n"
        "buffer_load_dwordx4 v26, s[20:23], s77 offen lds\n"
        "buffer_load_dwordx4 v27, s[20:23], s77 offen offset:1024 lds\n"
        "s_waitcnt vmcnt(9)\n"
        "ds_read_b64_tr_b16 a[160:161], v32 offset:2048\n"
        "ds_read_b64_tr_b16 a[164:165], v32 offset:2056\n"
        "ds_read_b64_tr_b16 a[168:169], v33 offset:2048\n"
        "ds_read_b64_tr_b16 a[172:173], v33 offset:2056\n"
        "ds_read_b128 v[212:215], v13 offset:640\n"
        "s_waitcnt lgkmcnt(5)\n"
        "v_mfma_f32_16x16x32_bf16 v[44:47], v[216:219], a[144:147], v[44:47]\n"
        "v_mfma_f32_16x16x32_bf16 v[48:51], v[216:219], a[148:151], v[48:51]\n"
        "v_mfma_f32_16x16x32_bf16 v[52:55], v[216:219], a[152:155], v[52:55]\n"
        "v_mfma_f32_16x16x32_bf16 v[56:59], v[216:219], a[156:159], v[56:59]\n"
        "s_add_u32 m0, s75, 0x1800\n"
        "s_add_u32 s77, s77, 0x3800\n"
        "buffer_load_dwordx4 v26, s[20:23], s77 offen lds\n"
        "buffer_load_dwordx4 v27, s[20:23], s77 offen offset:1024 lds\n"
        "s_waitcnt vmcnt(8)\n"
        "ds_read_b64_tr_b16 a[162:163], v30 offset:2048\n"
        "ds_read_b64_tr_b16 a[166:167], v30 offset:2056\n"
        "ds_read_b64_tr_b16 a[170:171], v31 offset:2048\n"
        "ds_read_b64_tr_b16 a[174:175], v31 offset:2056\n"
        "s_add_u32 m0, s75, 0x2000\n"
        "s_add_u32 s77, s77, 0x18800\n"
        "buffer_load_dwordx4 v26, s[20:23], s77 offen lds\n"
        "buffer_load_dwordx4 v27, s[20:23], s77 offen offset:1024 lds\n"
        "s_waitcnt vmcnt(8)\n"
        "ds_read_b64_tr_b16 a[176:177], v30 offset:4096\n"
        "ds_read_b64_tr_b16 a[180:181], v30 offset:4104\n"
        "ds_read_b64_tr_b16 a[184:185], v31 offset:4096\n"
        "ds_read_b64_tr_b16 a[188:189], v31 offset:4104\n"
        "ds_read_b128 v[216:219], v13 offset:704\n"
        "s_waitcnt lgkmcnt(5)\n"
        "v_mfma_f32_16x16x32_bf16 v[44:47], v[212:215], a[160:163], v[44:47]\n"
        "v_mfma_f32_16x16x32_bf16 v[48:51], v[212:215], a[164:167], v[48:51]\n"
        "v_mfma_f32_16x16x32_bf16 v[52:55], v[212:215], a[168:171], v[52:55]\n"
        "v_mfma_f32_16x16x32_bf16 v[56:59], v[212:215], a[172:175], v[56:59]\n"
        "s_add_u32 m0, s75, 0x2800\n"
        "s_add_u32 s77, s77, 0x3800\n"
        "buffer_load_dwordx4 v26, s[20:23], s77 offen lds\n"
        "buffer_load_dwordx4 v27, s[20:23], s77 offen offset:1024 lds\n"
        "s_waitcnt vmcnt(8)\n"
        "ds_read_b64_tr_b16 a[178:179], v30\n"
        "ds_read_b64_tr_b16 a[182:183], v30 offset:8\n"
        "ds_read_b64_tr_b16 a[186:187], v31\n"
        "ds_read_b64_tr_b16 a[190:191], v31 offset:8\n"
        "s_add_u32 s77, s53, 0x7\n"
        "s_and_b32 s77, s77, 7\n"
        "s_mul_i32 s77, s77, 0x38000\n"
        "s_add_u32 m0, s75, 0x3000\n"
        "s_add_u32 s77, s77, s76\n"
        "buffer_load_dwordx4 v26, s[20:23], s77 offen lds\n"
        "buffer_load_dwordx4 v27, s[20:23], s77 offen offset:1024 lds\n"
        "s_waitcnt vmcnt(8)\n"
        "ds_read_b64_tr_b16 a[192:193], v32 offset:4096\n"
        "ds_read_b64_tr_b16 a[196:197], v32 offset:4104\n"
        "ds_read_b64_tr_b16 a[200:201], v33 offset:4096\n"
        "ds_read_b64_tr_b16 a[204:205], v33 offset:4104\n"
        "ds_read_b128 v[212:215], v13 offset:768\n"
        "s_waitcnt lgkmcnt(5)\n"
        "v_mfma_f32_16x16x32_bf16 v[44:47], v[216:219], a[176:179], v[44:47]\n"
        "v_mfma_f32_16x16x32_bf16 v[48:51], v[216:219], a[180:183], v[48:51]\n"
        "v_mfma_f32_16x16x32_bf16 v[52:55], v[216:219], a[184:187], v[52:55]\n"
        "v_mfma_f32_16x16x32_bf16 v[56:59], v[216:219], a[188:191], v[56:59]\n"
        "s_add_u32 m0, s75, 0x3800\n"
        "s_add_u32 s77, s77, 0x3800\n"
        "buffer_load_dwordx4 v26, s[20:23], s77 offen lds\n"
        "buffer_load_dwordx4 v27, s[20:23], s77 offen offset:1024 lds\n"
        "s_waitcnt vmcnt(8)\n"
        "ds_read_b64_tr_b16 a[194:195], v32 offset:6144\n"
        "ds_read_b64_tr_b16 a[198:199], v32 offset:6152\n"
        "ds_read_b64_tr_b16 a[202:203], v33 offset:6144\n"
        "ds_read_b64_tr_b16 a[206:207], v33 offset:6152\n"
        "s_add_u32 m0, s75, 0x0\n"
        "s_add_u32 s77, s77, 0x18800\n"
        "buffer_load_dwordx4 v26, s[20:23], s77 offen lds\n"
        "buffer_load_dwordx4 v27, s[20:23], s77 offen offset:1024 lds\n"
        "s_add_u32 s28, s65, 0x1800\n"
        "buffer_load_dwordx2 v[20:21], v11, s[40:43], s28 offen sc1\n"
        "s_waitcnt vmcnt(9)\n"
        "ds_read_b64_tr_b16 a[208:209], v32 offset:8192\n"
        "ds_read_b64_tr_b16 a[212:213], v32 offset:8200\n"
        "ds_read_b64_tr_b16 a[216:217], v33 offset:8192\n"
        "ds_read_b64_tr_b16 a[220:221], v33 offset:8200\n"
        "ds_read_b128 v[216:219], v13 offset:832\n"
        "s_waitcnt lgkmcnt(5)\n"
        "v_mfma_f32_16x16x32_bf16 v[44:47], v[212:215], a[192:195], v[44:47]\n"
        "v_mfma_f32_16x16x32_bf16 v[48:51], v[212:215], a[196:199], v[48:51]\n"
        "v_mfma_f32_16x16x32_bf16 v[52:55], v[212:215], a[200:203], v[52:55]\n"
        "v_mfma_f32_16x16x32_bf16 v[56:59], v[212:215], a[204:207], v[56:59]\n"
        "s_waitcnt vmcnt(7)\n"
        "ds_read_b64_tr_b16 a[210:211], v32 offset:10240\n"
        "ds_read_b64_tr_b16 a[214:215], v32 offset:10248\n"
        "ds_read_b64_tr_b16 a[218:219], v33 offset:10240\n"
        "ds_read_b64_tr_b16 a[222:223], v33 offset:10248\n"
        "s_waitcnt vmcnt(5)\n"
        "ds_read_b64_tr_b16 a[224:225], v32 offset:12288\n"
        "ds_read_b64_tr_b16 a[228:229], v32 offset:12296\n"
        "ds_read_b64_tr_b16 a[232:233], v33 offset:12288\n"
        "ds_read_b64_tr_b16 a[236:237], v33 offset:12296\n"
        "ds_read_b128 v[212:215], v13 offset:896\n"
        "s_waitcnt lgkmcnt(5)\n"
        "v_mfma_f32_16x16x32_bf16 v[44:47], v[216:219], a[208:211], v[44:47]\n"
        "v_mfma_f32_16x16x32_bf16 v[48:51], v[216:219], a[212:215], v[48:51]\n"
        "v_mfma_f32_16x16x32_bf16 v[52:55], v[216:219], a[216:219], v[52:55]\n"
        "v_mfma_f32_16x16x32_bf16 v[56:59], v[216:219], a[220:223], v[56:59]\n"
        "s_lshl_b32 s67, s50, 12\n"
        "s_lshl_b32 s55, s53, 1\n"
        "s_add_u32 s55, s55, s50\n"
        "s_add_u32 s55, s55, 0x0\n"
        "s_and_b32 s55, s55, 15\n"
        "s_mul_i32 s55, s55, 0x2000\n"
        "s_add_u32 s34, s55, s35\n"
        "s_lshl_b32 s55, s53, 1\n"
        "s_add_u32 s55, s55, s50\n"
        "s_add_u32 s55, s55, 0x4\n"
        "s_and_b32 s55, s55, 15\n"
        "s_mul_i32 s55, s55, 0x2000\n"
        "s_add_u32 s61, s55, s35\n"
        "s_lshl_b32 s55, s53, 1\n"
        "s_add_u32 s55, s55, s50\n"
        "s_add_u32 s55, s55, 0x8\n"
        "s_and_b32 s55, s55, 15\n"
        "s_mul_i32 s55, s55, 0x2000\n"
        "s_add_u32 s63, s55, s35\n"
        "s_lshl_b32 s55, s53, 1\n"
        "s_add_u32 s55, s55, s50\n"
        "s_add_u32 s55, s55, 0xc\n"
        "s_and_b32 s55, s55, 15\n"
        "s_mul_i32 s55, s55, 0x2000\n"
        "s_add_u32 s66, s55, s35\n"
        "s_lshl_b32 s55, s30, 11\n"
        "s_add_u32 s62, s55, s62\n"
        "s_mul_i32 s55, s29, 0x1000\n"
        "v_add_u32 v12, s55, v5\n"
        "v_xor_b32 v15, 0x1000, v12\n"
        "s_mul_i32 s69, s50, 0x1c0\n"
        "s_add_u32 s69, s69, s65\n"
        "v_lshlrev_b32 v6, 2, v0\n"
        "v_add_u32 v6, 0x10000, v6\n"
        "v_add_u32 v7, 0x10000, v14\n"
        "s_waitcnt vmcnt(3)\n"
        "ds_read_b64_tr_b16 a[226:227], v32 offset:14336\n"
        "ds_read_b64_tr_b16 a[230:231], v32 offset:14344\n"
        "ds_read_b64_tr_b16 a[234:235], v33 offset:14336\n"
        "ds_read_b64_tr_b16 a[238:239], v33 offset:14344\n"
        "s_waitcnt vmcnt(1)\n"
        "ds_read_b64_tr_b16 a[240:241], v32\n"
        "ds_read_b64_tr_b16 a[244:245], v32 offset:8\n"
        "ds_read_b64_tr_b16 a[248:249], v33\n"
        "ds_read_b64_tr_b16 a[252:253], v33 offset:8\n"
        "s_branch .Lku4\n"
        ".Lkt4:\n"
        "s_setpc_b64 s[86:87]\n"
        ".Lku4:\n"
        "ds_read_b128 v[216:219], v13 offset:960\n"
        "s_waitcnt lgkmcnt(5)\n"
        "v_mfma_f32_16x16x32_bf16 v[44:47], v[212:215], a[224:227], v[44:47]\n"
        "v_mfma_f32_16x16x32_bf16 v[48:51], v[212:215], a[228:231], v[48:51]\n"
        "v_mfma_f32_16x16x32_bf16 v[52:55], v[212:215], a[232:235], v[52:55]\n"
        "v_mfma_f32_16x16x32_bf16 v[56:59], v[212:215], a[236:239], v[56:59]\n"
        "s_waitcnt vmcnt(0)\n"
        "v_cmp_ne_u64 vcc, s[56:57], v[20:21]\n"
        "s_nop 0\n"
        "s_cbranch_vccnz .Lkp4\n"
        ".Lkq4:\n"
        "s_barrier\n"
        "v_and_b32 v11, 7, v0\n"
        "s_add_u32 m0, s67, 0x0\n"
        "s_nop 0\n"
        "buffer_load_dwordx4 v5, s[40:43], s34 offen sc1 lds\n"
        "s_addk_i32 s34, 1024\n"
        "buffer_load_dwordx4 v5, s[40:43], s34 offen offset:1024 sc1 lds\n"
        "s_addk_i32 s34, 1024\n"
        "buffer_load_dwordx4 v5, s[40:43], s34 offen offset:2048 sc1 lds\n"
        "s_addk_i32 s34, 1024\n"
        "buffer_load_dwordx4 v5, s[40:43], s34 offen offset:3072 sc1 lds\n"
        "s_add_u32 m0, s67, 0x4000\n"
        "s_nop 0\n"
        "buffer_load_dwordx4 v5, s[40:43], s61 offen sc1 lds\n"
        "s_addk_i32 s61, 1024\n"
        "buffer_load_dwordx4 v5, s[40:43], s61 offen offset:1024 sc1 lds\n"
        "s_addk_i32 s61, 1024\n"
        "buffer_load_dwordx4 v5, s[40:43], s61 offen offset:2048 sc1 lds\n"
        "s_addk_i32 s61, 1024\n"
        "buffer_load_dwordx4 v5, s[40:43], s61 offen offset:3072 sc1 lds\n"
        "v_min_u32 v11, 6, v11\n"
        "v_lshlrev_b32 v11, 6, v11\n"
        "s_waitcnt vmcnt(4)\n"
        "s_barrier\n"
        "ds_read_b128 v[108:111], v2\n"
        "ds_read_b128 v[112:115], v2 offset:1024\n"
        "ds_read_b128 v[116:119], v2 offset:2048\n"
        "ds_read_b128 v[120:123], v2 offset:3072\n"
        "ds_read_b128 v[124:127], v2 offset:4096\n"
        "ds_read_b128 v[128:131], v2 offset:5120\n"
        "s_waitcnt lgkmcnt(4)\n"
        "v_mfma_f32_16x16x32_bf16 v[76:79], a[0:3], v[108:111], 0\n"
        "ds_read_b128 v[132:135], v2 offset:6144\n"
        "s_add_u32 m0, s67, 0x8000\n"
        "v_mfma_f32_16x16x32_bf16 v[80:83], a[4:7], v[108:111], 0\n"
        "buffer_load_dwordx4 v5, s[40:43], s63 offen sc1 lds\n"
        "v_mfma_f32_16x16x32_bf16 v[84:87], a[0:3], v[112:115], 0\n"
        "ds_read_b128 v[136:139], v2 offset:7168\n"
        "v_mfma_f32_16x16x32_bf16 v[88:91], a[4:7], v[112:115], 0\n"
        "s_waitcnt lgkmcnt(4)\n"
        "v_mfma_f32_16x16x32_bf16 v[92:95], a[0:3], v[116:119], 0\n"
        "ds_read_b128 v[108:111], v2 offset:8192\n"
        "s_add_u32 m0, s67, 0x8000\n"
        "v_mfma_f32_16x16x32_bf16 v[96:99], a[4:7], v[116:119], 0\n"
        "s_addk_i32 s63, 1024\n"
        "buffer_load_dwordx4 v5, s[40:43], s63 offen offset:1024 sc1 lds\n"
        "v_mfma_f32_16x16x32_bf16 v[100:103], a[0:3], v[120:123], 0\n"
        "ds_read_b128 v[112:115], v2 offset:9216\n"
        "v_mfma_f32_16x16x32_bf16 v[104:107], a[4:7], v[120:123], 0\n"
        "s_waitcnt lgkmcnt(4)\n"
        "v_mfma_f32_16x16x32_bf16 v[76:79], a[16:19], v[124:127], v[76:79]\n"
        "ds_read_b128 v[116:119], v2 offset:10240\n"
        "s_add_u32 m0, s67, 0x8000\n"
        "v_mfma_f32_16x16x32_bf16 v[80:83], a[20:23], v[124:127], v[80:83]\n"
        "s_addk_i32 s63, 1024\n"
        "buffer_load_dwordx4 v5, s[40:43], s63 offen offset:2048 sc1 lds\n"
        "v_mfma_f32_16x16x32_bf16 v[84:87], a[16:19], v[128:131], v[84:87]\n"
        "ds_read_b128 v[120:123], v2 offset:11264\n"
        "v_mfma_f32_16x16x32_bf16 v[88:91], a[20:23], v[128:131], v[88:91]\n"
        "s_waitcnt lgkmcnt(4)\n"
        "v_mfma_f32_16x16x32_bf16 v[92:95], a[16:19], v[132:135], v[92:95]\n"
        "ds_read_b128 v[124:127], v2 offset:12288\n"
        "s_add_u32 m0, s67, 0x8000\n"
        "v_mfma_f32_16x16x32_bf16 v[96:99], a[20:23], v[132:135], v[96:99]\n"
        "s_addk_i32 s63, 1024\n"
        "buffer_load_dwordx4 v5, s[40:43], s63 offen offset:3072 sc1 lds\n"
        "v_mfma_f32_16x16x32_bf16 v[100:103], a[16:19], v[136:139], v[100:103]\n"
        "ds_read_b128 v[128:131], v2 offset:13312\n"
        "v_mfma_f32_16x16x32_bf16 v[104:107], a[20:23], v[136:139], v[104:107]\n"
        "s_waitcnt lgkmcnt(4)\n"
        "v_mfma_f32_16x16x32_bf16 v[76:79], a[32:35], v[108:111], v[76:79]\n"
        "ds_read_b128 v[132:135], v2 offset:14336\n"
        "s_add_u32 m0, s67, 0xc000\n"
        "v_mfma_f32_16x16x32_bf16 v[80:83], a[36:39], v[108:111], v[80:83]\n"
        "buffer_load_dwordx4 v5, s[40:43], s66 offen sc1 lds\n"
        "v_mfma_f32_16x16x32_bf16 v[84:87], a[32:35], v[112:115], v[84:87]\n"
        "ds_read_b128 v[136:139], v2 offset:15360\n"
        "v_mfma_f32_16x16x32_bf16 v[88:91], a[36:39], v[112:115], v[88:91]\n"
        "s_waitcnt vmcnt(5)\n"
        "s_barrier\n"
        "s_waitcnt lgkmcnt(4)\n"
        "v_mfma_f32_16x16x32_bf16 v[92:95], a[32:35], v[116:119], v[92:95]\n"
        "ds_read_b128 v[108:111], v2 offset:16384\n"
        "s_add_u32 m0, s67, 0xc000\n"
        "v_mfma_f32_16x16x32_bf16 v[96:99], a[36:39], v[116:119], v[96:99]\n"
        "s_addk_i32 s66, 1024\n"
        "buffer_load_dwordx4 v5, s[40:43], s66 offen offset:1024 sc1 lds\n"
        "v_mfma_f32_16x16x32_bf16 v[100:103], a[32:35], v[120:123], v[100:103]\n"
        "ds_read_b128 v[112:115], v2 offset:17408\n"
        "v_mfma_f32_16x16x32_bf16 v[104:107], a[36:39], v[120:123], v[104:107]\n"
        "s_waitcnt lgkmcnt(4)\n"
        "v_mfma_f32_16x16x32_bf16 v[76:79], a[48:51], v[124:127], v[76:79]\n"
        "ds_read_b128 v[116:119], v2 offset:18432\n"
        "s_add_u32 m0, s67, 0xc000\n"
        "v_mfma_f32_16x16x32_bf16 v[80:83], a[52:55], v[124:127], v[80:83]\n"
        "s_addk_i32 s66, 1024\n"
        "buffer_load_dwordx4 v5, s[40:43], s66 offen offset:2048 sc1 lds\n"
        "v_mfma_f32_16x16x32_bf16 v[84:87], a[48:51], v[128:131], v[84:87]\n"
        "ds_read_b128 v[120:123], v2 offset:19456\n"
        "v_mfma_f32_16x16x32_bf16 v[88:91], a[52:55], v[128:131], v[88:91]\n"
        "s_waitcnt lgkmcnt(4)\n"
        "v_mfma_f32_16x16x32_bf16 v[92:95], a[48:51], v[132:135], v[92:95]\n"
        "ds_read_b128 v[124:127], v2 offset:20480\n"
        "s_add_u32 m0, s67, 0xc000\n"
        "v_mfma_f32_16x16x32_bf16 v[96:99], a[52:55], v[132:135], v[96:99]\n"
        "s_addk_i32 s66, 1024\n"
        "buffer_load_dwordx4 v5, s[40:43], s66 offen offset:3072 sc1 lds\n"
        "v_mfma_f32_16x16x32_bf16 v[100:103], a[48:51], v[136:139], v[100:103]\n"
        "ds_read_b128 v[128:131], v2 offset:21504\n"
        "v_mfma_f32_16x16x32_bf16 v[104:107], a[52:55], v[136:139], v[104:107]\n"
        "s_waitcnt lgkmcnt(4)\n"
        "v_mfma_f32_16x16x32_bf16 v[76:79], a[64:67], v[108:111], v[76:79]\n"
        "ds_read_b128 v[132:135], v2 offset:22528\n"
        "s_add_u32 m0, s75, 0x800\n"
        "v_mfma_f32_16x16x32_bf16 v[80:83], a[68:71], v[108:111], v[80:83]\n"
        "s_add_u32 s77, s77, 0x3800\n"
        "buffer_load_dwordx4 v26, s[20:23], s77 offen lds\n"
        "v_mfma_f32_16x16x32_bf16 v[84:87], a[64:67], v[112:115], v[84:87]\n"
        "ds_read_b128 v[136:139], v2 offset:23552\n"
        "v_mfma_f32_16x16x32_bf16 v[88:91], a[68:71], v[112:115], v[88:91]\n"
        "s_waitcnt lgkmcnt(4)\n"
        "v_mfma_f32_16x16x32_bf16 v[92:95], a[64:67], v[116:119], v[92:95]\n"
        "ds_read_b128 v[108:111], v2 offset:24576\n"
        "s_add_u32 m0, s75, 0x800\n"
        "v_mfma_f32_16x16x32_bf16 v[96:99], a[68:71], v[116:119], v[96:99]\n"
        "buffer_load_dwordx4 v27, s[20:23], s77 offen offset:1024 lds\n"
        "v_mfma_f32_16x16x32_bf16 v[100:103], a[64:67], v[120:123], v[100:103]\n"
        "ds_read_b128 v[112:115], v2 offset:25600\n"
        "v_mfma_f32_16x16x32_bf16 v[104:107], a[68:71], v[120:123], v[104:107]\n"
        "s_waitcnt lgkmcnt(4)\n"
        "v_mfma_f32_16x16x32_bf16 v[76:79], a[80:83], v[124:127], v[76:79]\n"
        "ds_read_b128 v[116:119], v2 offset:26624\n"
        "buffer_load_dword v244, v14, s[40:43], s62 offen sc1\n"
        "v_mfma_f32_16x16x32_bf16 v[80:83], a[84:87], v[124:127], v[80:83]\n"
        "v_mfma_f32_16x16x32_bf16 v[84:87], a[80:83], v[128:131], v[84:87]\n"
        "ds_read_b128 v[120:123], v2 offset:27648\n"
        "buffer_load_dword v245, v14, s[40:43], s62 offen offset:256 sc1\n"
        "v_mfma_f32_16x16x32_bf16 v[88:91], a[84:87], v[128:131], v[88:91]\n"
        "s_waitcnt lgkmcnt(4)\n"
        "v_mfma_f32_16x16x32_bf16 v[92:95], a[80:83], v[132:135], v[92:95]\n"
        "ds_read_b128 v[124:127], v2 offset:28672\n"
        "buffer_load_dword v246, v14, s[40:43], s62 offen offset:512 sc1\n"
        "v_mfma_f32_16x16x32_bf16 v[96:99], a[84:87], v[132:135], v[96:99]\n"
        "v_mfma_f32_16x16x32_bf16 v[100:103], a[80:83], v[136:139], v[100:103]\n"
        "ds_read_b128 v[128:131], v2 offset:29696\n"
        "buffer_load_dword v247, v14, s[40:43], s62 offen offset:768 sc1\n"
        "v_mfma_f32_16x16x32_bf16 v[104:107], a[84:87], v[136:139], v[104:107]\n"
        "s_waitcnt lgkmcnt(4)\n"
        "v_mfma_f32_16x16x32_bf16 v[76:79], a[96:99], v[108:111], v[76:79]\n"
        "ds_read_b128 v[132:135], v2 offset:30720\n"
        "buffer_load_dword v248, v14, s[40:43], s62 offen offset:1024 sc1\n"
        "v_mfma_f32_16x16x32_bf16 v[80:83], a[100:103], v[108:111], v[80:83]\n"
        "v_mfma_f32_16x16x32_bf16 v[84:87], a[96:99], v[112:115], v[84:87]\n"
        "ds_read_b128 v[136:139], v2 offset:31744\n"
        "buffer_load_dword v249, v14, s[40:43], s62 offen offset:1280 sc1\n"
        "v_mfma_f32_16x16x32_bf16 v[88:91], a[100:103], v[112:115], v[88:91]\n"
        "s_waitcnt vmcnt(12)\n"
        "s_barrier\n"
        "s_waitcnt lgkmcnt(4)\n"
        "v_mfma_f32_16x16x32_bf16 v[92:95], a[96:99], v[116:119], v[92:95]\n"
        "ds_read_b128 v[108:111], v2 offset:32768\n"
        "buffer_load_dword v250, v14, s[40:43], s62 offen offset:1536 sc1\n"
        "v_mfma_f32_16x16x32_bf16 v[96:99], a[100:103], v[116:119], v[96:99]\n"
        "v_mfma_f32_16x16x32_bf16 v[100:103], a[96:99], v[120:123], v[100:103]\n"
        "ds_read_b128 v[112:115], v2 offset:33792\n"
        "buffer_load_dword v251, v14, s[40:43], s62 offen offset:1792 sc1\n"
        "v_mfma_f32_16x16x32_bf16 v[104:107], a[100:103], v[120:123], v[104:107]\n"
        "s_waitcnt lgkmcnt(4)\n"
        "v_mfma_f32_16x16x32_bf16 v[76:79], a[112:115], v[124:127], v[76:79]\n"
        "ds_read_b128 v[116:119], v2 offset:34816\n"
        "s_mul_i32 s77, s60, 0x8\n"
        "v_mfma_f32_16x16x32_bf16 v[80:83], a[116:119], v[124:127], v[80:83]\n"
        "s_add_u32 s77, s77, s80\n"
        "s_add_u32 m0, s74, 0x800\n"
        "v_mfma_f32_16x16x32_bf16 v[84:87], a[112:115], v[128:131], v[84:87]\n"
        "ds_read_b128 v[120:123], v2 offset:35840\n"
        "s_add_u32 s77, s77, 0x0\n"
        "v_mfma_f32_16x16x32_bf16 v[88:91], a[116:119], v[128:131], v[88:91]\n"
        "buffer_load_dwordx4 v28, s[24:27], s77 offen lds\n"
        "s_waitcnt lgkmcnt(4)\n"
        "v_mfma_f32_16x16x32_bf16 v[92:95], a[112:115], v[132:135], v[92:95]\n"
        "ds_read_b128 v[124:127], v2 offset:36864\n"
        "s_add_u32 m0, s74, 0x800\n"
        "v_mfma_f32_16x16x32_bf16 v[96:99], a[116:119], v[132:135], v[96:99]\n"
        "buffer_load_dwordx4 v29, s[24:27], s77 offen offset:1024 lds\n"
        "v_mfma_f32_16x16x32_bf16 v[100:103], a[112:115], v[136:139], v[100:103]\n"
        "ds_read_b128 v[128:131], v2 offset:37888\n"
        "v_mfma_f32_16x16x32_bf16 v[104:107], a[116:119], v[136:139], v[104:107]\n"
        "s_waitcnt lgkmcnt(4)\n"
        "v_mfma_f32_16x16x32_bf16 v[76:79], a[128:131], v[108:111], v[76:79]\n"
        "ds_read_b128 v[132:135], v2 offset:38912\n"
        "s_add_u32 m0, s74, 0x1000\n"
        "v_mfma_f32_16x16x32_bf16 v[80:83], a[132:135], v[108:111], v[80:83]\n"
        "s_add_u32 s77, s77, 0x1000\n"
        "buffer_load_dwordx4 v28, s[24:27], s77 offen lds\n"
        "v_mfma_f32_16x16x32_bf16 v[84:87], a[128:131], v[112:115], v[84:87]\n"
        "ds_read_b128 v[136:139], v2 offset:39936\n"
        "v_mfma_f32_16x16x32_bf16 v[88:91], a[132:135], v[112:115], v[88:91]\n"
        "s_waitcnt lgkmcnt(4)\n"
        "v_mfma_f32_16x16x32_bf16 v[92:95], a[128:131], v[116:119], v[92:95]\n"
        "ds_read_b128 v[108:111], v2 offset:40960\n"
        "s_add_u32 m0, s74, 0x1000\n"
        "v_mfma_f32_16x16x32_bf16 v[96:99], a[132:135], v[116:119], v[96:99]\n"
        "buffer_load_dwordx4 v29, s[24:27], s77 offen offset:1024 lds\n"
        "v_mfma_f32_16x16x32_bf16 v[100:103], a[128:131], v[120:123], v[100:103]\n"
        "ds_read_b128 v[112:115], v2 offset:41984\n"
        "v_mfma_f32_16x16x32_bf16 v[104:107], a[132:135], v[120:123], v[104:107]\n"
        "s_waitcnt lgkmcnt(4)\n"
        "v_mfma_f32_16x16x32_bf16 v[76:79], a[144:147], v[124:127], v[76:79]\n"
        "ds_read_b128 v[116:119], v2 offset:43008\n"
        "s_mul_i32 s77, s68, 0x8\n"
        "v_mfma_f32_16x16x32_bf16 v[80:83], a[148:151], v[124:127], v[80:83]\n"
        "s_add_u32 s77, s77, s80\n"
        "s_add_u32 m0, s74, 0x0\n"
        "v_mfma_f32_16x16x32_bf16 v[84:87], a[144:147], v[128:131], v[84:87]\n"
        "ds_read_b128 v[120:123], v2 offset:44032\n"
        "s_add_u32 s77, s77, 0x0\n"
        "v_mfma_f32_16x16x32_bf16 v[88:91], a[148:151], v[128:131], v[88:91]\n"
        "buffer_load_dwordx4 v28, s[24:27], s77 offen lds\n"
        "s_waitcnt lgkmcnt(4)\n"
        "v_mfma_f32_16x16x32_bf16 v[92:95], a[144:147], v[132:135], v[92:95]\n"
        "ds_read_b128 v[124:127], v2 offset:45056\n"
        "s_add_u32 m0, s74, 0x0\n"
        "v_mfma_f32_16x16x32_bf16 v[96:99], a[148:151], v[132:135], v[96:99]\n"
        "buffer_load_dwordx4 v29, s[24:27], s77 offen offset:1024 lds\n"
        "v_mfma_f32_16x16x32_bf16 v[100:103], a[144:147], v[136:139], v[100:103]\n"
        "ds_read_b128 v[128:131], v2 offset:46080\n"
        "v_mfma_f32_16x16x32_bf16 v[104:107], a[148:151], v[136:139], v[104:107]\n"
        "s_waitcnt lgkmcnt(4)\n"
        "v_mfma_f32_16x16x32_bf16 v[76:79], a[160:163], v[108:111], v[76:79]\n"
        "ds_read_b128 v[132:135], v2 offset:47104\n"
        "s_add_u32 m0, s75, 0x1000\n"
        "v_mfma_f32_16x16x32_bf16 v[80:83], a[164:167], v[108:111], v[80:83]\n"
        "s_add_u32 s77, s77, 0x1000\n"
        "buffer_load_dwordx4 v28, s[24:27], s77 offen lds\n"
        "v_mfma_f32_16x16x32_bf16 v[84:87], a[160:163], v[112:115], v[84:87]\n"
        "ds_read_b128 v[136:139], v2 offset:48128\n"
        "v_mfma_f32_16x16x32_bf16 v[88:91], a[164:167], v[112:115], v[88:91]\n"
        "s_waitcnt vmcnt(17)\n"
        "s_barrier\n"
        "s_waitcnt lgkmcnt(4)\n"
        "v_mfma_f32_16x16x32_bf16 v[92:95], a[160:163], v[116:119], v[92:95]\n"
        "ds_read_b128 v[108:111], v2 offset:49152\n"
        "s_add_u32 m0, s75, 0x1000\n"
        "v_mfma_f32_16x16x32_bf16 v[96:99], a[164:167], v[116:119], v[96:99]\n"
        "buffer_load_dwordx4 v29, s[24:27], s77 offen offset:1024 lds\n"
        "s_waitcnt vmcnt(8)\n"
        "v_mfma_f32_16x16x32_bf16 v[100:103], a[160:163], v[120:123], v[100:103]\n"
        "ds_read_b128 v[112:115], v2 offset:50176\n"
        "v_add_f32 v244, v244, v245\n"
        "v_mfma_f32_16x16x32_bf16 v[104:107], a[164:167], v[120:123], v[104:107]\n"
        "v_add_f32 v246, v246, v247\n"
        "v_add_f32 v248, v248, v249\n"
        "v_add_f32 v250, v250, v251\n"
        "v_add_f32 v244, v244, v246\n"
        "v_add_f32 v248, v248, v250\n"
        "v_add_f32 v244, v244, v248\n"
        "ds_write_b32 v6, v244\n"
        "s_waitcnt lgkmcnt(5)\n"
        "v_mfma_f32_16x16x32_bf16 v[76:79], a[176:179], v[124:127], v[76:79]\n"
        "ds_read_b128 v[116:119], v2 offset:51200\n"
        "s_mul_i32 s77, s81, 0x8\n"
        "v_mfma_f32_16x16x32_bf16 v[80:83], a[180:183], v[124:127], v[80:83]\n"
        "s_add_u32 s77, s77, s80\n"
        "s_add_u32 m0, s75, 0x1800\n"
        "v_mfma_f32_16x16x32_bf16 v[84:87], a[176:179], v[128:131], v[84:87]\n"
        "ds_read_b128 v[120:123], v2 offset:52224\n"
        "s_add_u32 s77, s77, 0x0\n"
        "v_mfma_f32_16x16x32_bf16 v[88:91], a[180:183], v[128:131], v[88:91]\n"
        "buffer_load_dwordx4 v28, s[24:27], s77 offen lds\n"
        "s_waitcnt lgkmcnt(5)\n"
        "v_mfma_f32_16x16x32_bf16 v[92:95], a[176:179], v[132:135], v[92:95]\n"
        "ds_read_b128 v[124:127], v2 offset:53248\n"
        "s_add_u32 m0, s75, 0x1800\n"
        "v_mfma_f32_16x16x32_bf16 v[96:99], a[180:183], v[132:135], v[96:99]\n"
        "buffer_load_dwordx4 v29, s[24:27], s77 offen offset:1024 lds\n"
        "v_mfma_f32_16x16x32_bf16 v[100:103], a[176:179], v[136:139], v[100:103]\n"
        "ds_read_b128 v[128:131], v2 offset:54272\n"
        "v_mfma_f32_16x16x32_bf16 v[104:107], a[180:183], v[136:139], v[104:107]\n"
        "s_waitcnt lgkmcnt(4)\n"
        "s_barrier\n"
        "v_mfma_f32_16x16x32_bf16 v[76:79], a[192:195], v[108:111], v[76:79]\n"
        "ds_read_b128 v[132:135], v2 offset:55296\n"
        "s_add_u32 m0, s75, 0x2000\n"
        "v_mfma_f32_16x16x32_bf16 v[80:83], a[196:199], v[108:111], v[80:83]\n"
        "s_add_u32 s77, s77, 0x1000\n"
        "buffer_load_dwordx4 v28, s[24:27], s77 offen lds\n"
        "v_mfma_f32_16x16x32_bf16 v[84:87], a[192:195], v[112:115], v[84:87]\n"
        "ds_read_b128 v[136:139], v2 offset:56320\n"
        "ds_read_b32 v244, v7\n"
        "v_mfma_f32_16x16x32_bf16 v[88:91], a[196:199], v[112:115], v[88:91]\n"
        "ds_read_b32 v245, v7 offset:256\n"
        "ds_read_b32 v246, v7 offset:512\n"
        "s_waitcnt lgkmcnt(7)\n"
        "v_mfma_f32_16x16x32_bf16 v[92:95], a[192:195], v[116:119], v[92:95]\n"
        "ds_read_b128 v[108:111], v2 offset:57344\n"
        "ds_read_b32 v247, v7 offset:768\n"
        "v_mfma_f32_16x16x32_bf16 v[96:99], a[196:199], v[116:119], v[96:99]\n"
        "v_mov_b32 v199, 0\n"
        "v_mov_b32 v203, 0\n"
        "v_mfma_f32_16x16x32_bf16 v[100:103], a[192:195], v[120:123], v[100:103]\n"
        "ds_read_b128 v[112:115], v2 offset:58368\n"
        "v_mov_b32 v207, 0\n"
        "v_mfma_f32_16x16x32_bf16 v[104:107], a[196:199], v[120:123], v[104:107]\n"
        "v_mov_b32 v211, 0\n"
        "s_waitcnt lgkmcnt(1)\n"
        "v_add_f32 v244, v244, v246\n"
        "v_add_f32 v245, v245, v247\n"
        "v_mul_f32 v244, 0x3b000000, v244\n"
        "v_mul_f32 v245, 0x3b000000, v245\n"
        "v_mov_b32 v246, v244\n"
        "v_mov_b32 v247, v245\n"
        "v_cmp_eq_u32 vcc, 0, v3\n"
        "v_permlane32_swap_b32 v244, v246\n"
        "v_permlane32_swap_b32 v245, v247\n"
        "v_fma_f32 v246, -v244, v244, v246\n"
        "v_fma_f32 v247, -v245, v245, v247\n"
        "v_max_f32 v246, 0, v246\n"
        "v_max_f32 v247, 0, v247\n"
        "v_add_f32 v246, 0x3727c5ac, v246\n"
        "v_add_f32 v247, 0x3727c5ac, v247\n"
        "v_rsq_f32 v16, v246\n"
        "v_rsq_f32 v17, v247\n"
        "v_xor_b32 v244, 0x80000000, v244\n"
        "v_xor_b32 v245, 0x80000000, v245\n"
        "v_mul_f32 v246, v246, v16\n"
        "v_mul_f32 v247, v247, v17\n"
        "v_cvt_pk_bf16_f32 v202, v244, v246\n"
        "v_cvt_pk_bf16_f32 v210, v245, v247\n"
        "v_lshlrev_b32 v248, 16, v202\n"
        "v_lshlrev_b32 v250, 16, v210\n"
        "v_and_b32 v249, 0xffff0000, v202\n"
        "v_and_b32 v251, 0xffff0000, v210\n"
        "v_sub_f32 v248, v244, v248\n"
        "v_sub_f32 v250, v245, v250\n"
        "v_sub_f32 v249, v246, v249\n"
        "v_sub_f32 v251, v247, v251\n"
        "v_cvt_pk_bf16_f32 v248, v248, v249\n"
        "v_cvt_pk_bf16_f32 v250, v250, v251\n"
        "v_cndmask_b32 v196, 0, v202, vcc\n"
        "v_cndmask_b32 v204, 0, v210, vcc\n"
        "v_cndmask_b32 v197, 0, v248, vcc\n"
        "v_cndmask_b32 v205, 0, v250, vcc\n"
        "v_mov_b32 v198, v196\n"
        "v_mov_b32 v206, v204\n"
        "v_cmp_eq_u32 vcc, 16, v3\n"
        "s_nop 1\n"
        "v_cndmask_b32 v201, 0, v248, vcc\n"
        "v_cndmask_b32 v209, 0, v250, vcc\n"
        "v_cndmask_b32 v200, 0, v202, vcc\n"
        "v_cndmask_b32 v208, 0, v210, vcc\n"
        "v_mov_b32 v202, v200\n"
        "v_mov_b32 v210, v208\n"
        "s_cmp_eq_u32 s29, 1\n"
        "s_cselect_b64 vcc, -1, 0\n"
        "v_cndmask_b32 v16, v16, v17, vcc\n"
        "v_mov_b32 v17, v16\n"
        "s_nop 1\n"
        "v_permlane16_swap_b32 v16, v17\n"
        "ds_read_b64_tr_b16 a[242:243], v32 offset:2048\n"
        "ds_read_b64_tr_b16 a[246:247], v32 offset:2056\n"
        "ds_read_b64_tr_b16 a[250:251], v33 offset:2048\n"
        "ds_read_b64_tr_b16 a[254:255], v33 offset:2056\n"
        "s_waitcnt lgkmcnt(0)\n"
        "v_mfma_f32_16x16x32_bf16 v[44:47], v[216:219], a[240:243], v[44:47]\n"
        "v_mfma_f32_16x16x32_bf16 v[48:51], v[216:219], a[244:247], v[48:51]\n"
        "v_mfma_f32_16x16x32_bf16 v[52:55], v[216:219], a[248:251], v[52:55]\n"
        "v_mfma_f32_16x16x32_bf16 v[56:59], v[216:219], a[252:255], v[56:59]\n"
        "v_mfma_f32_16x16x32_bf16 v[76:79], a[208:211], v[124:127], v[76:79]\n"
        "ds_read_b128 v[116:119], v2 offset:59392\n"
        "s_add_u32 m0, s75, 0x2000\n"
        "v_mfma_f32_16x16x32_bf16 v[80:83], a[212:215], v[124:127], v[80:83]\n"
        "buffer_load_dwordx4 v29, s[24:27], s77 offen offset:1024 lds\n"
        "s_mul_i32 s77, s82, 0x8\n"
        "v_mfma_f32_16x16x32_bf16 v[84:87], a[208:211], v[128:131], v[84:87]\n"
        "ds_read_b128 v[120:123], v2 offset:60416\n"
        "s_add_u32 s77, s77, s80\n"
        "v_mfma_f32_16x16x32_bf16 v[88:91], a[212:215], v[128:131], v[88:91]\n"
        "s_add_u32 m0, s75, 0x2800\n"
        "s_add_u32 s77, s77, 0x0\n"
        "v_mfma_f32_16x16x32_bf16 v[92:95], a[208:211], v[132:135], v[92:95]\n"
        "ds_read_b128 v[124:127], v2 offset:61440\n"
        "buffer_load_dwordx4 v28, s[24:27], s77 offen lds\n"
        "v_mfma_f32_16x16x32_bf16 v[96:99], a[212:215], v[132:135], v[96:99]\n"
        "s_add_u32 m0, s75, 0x2800\n"
        "s_nop 0\n"
        "buffer_load_dwordx4 v29, s[24:27], s77 offen offset:1024 lds\n"
        "v_mfma_f32_16x16x32_bf16 v[100:103], a[208:211], v[136:139], v[100:103]\n"
        "ds_read_b128 v[128:131], v2 offset:62464\n"
        "s_lshl_b32 s55, s30, 6\n"
        "v_mfma_f32_16x16x32_bf16 v[104:107], a[212:215], v[136:139], v[104:107]\n"
        "v_xor_b32 v30, s55, v30\n"
        "v_xor_b32 v31, s55, v31\n"
        "v_mfma_f32_16x16x32_bf16 v[76:79], a[224:227], v[108:111], v[76:79]\n"
        "ds_read_b128 v[132:135], v2 offset:63488\n"
        "v_xor_b32 v32, s55, v32\n"
        "v_mfma_f32_16x16x32_bf16 v[80:83], a[228:231], v[108:111], v[80:83]\n"
        "v_xor_b32 v33, s55, v33\n"
        "v_mov_b32 v183, 0\n"
        "v_mfma_f32_16x16x32_bf16 v[84:87], a[224:227], v[112:115], v[84:87]\n"
        "ds_read_b128 v[136:139], v2 offset:64512\n"
        "v_mov_b32 v187, 0\n"
        "v_mfma_f32_16x16x32_bf16 v[88:91], a[228:231], v[112:115], v[88:91]\n"
        "v_mov_b32 v191, 0\n"
        "v_mov_b32 v195, 0\n"
        "s_waitcnt lgkmcnt(4)\n"
        "v_mfma_f32_16x16x32_bf16 v[92:95], a[224:227], v[116:119], v[92:95]\n"
        "ds_read_b128 v[108:111], v2\n"
        "v_cvt_pk_bf16_f32 v180, v44, v45\n"
        "v_mfma_f32_16x16x32_bf16 v[96:99], a[228:231], v[116:119], v[96:99]\n"
        "v_cvt_pk_bf16_f32 v184, v48, v49\n"
        "v_cvt_pk_bf16_f32 v188, v52, v53\n"
        "v_mfma_f32_16x16x32_bf16 v[100:103], a[224:227], v[120:123], v[100:103]\n"
        "ds_read_b128 v[112:115], v2 offset:1024\n"
        "v_cvt_pk_bf16_f32 v192, v56, v57\n"
        "v_mfma_f32_16x16x32_bf16 v[104:107], a[228:231], v[120:123], v[104:107]\n"
        "v_lshlrev_b32 v46, 16, v180\n"
        "v_lshlrev_b32 v50, 16, v184\n"
        "s_waitcnt lgkmcnt(4)\n"
        "v_mfma_f32_16x16x32_bf16 v[76:79], a[240:243], v[124:127], v[76:79]\n"
        "ds_read_b128 v[116:119], v2 offset:2048\n"
        "v_lshlrev_b32 v54, 16, v188\n"
        "v_mfma_f32_16x16x32_bf16 v[80:83], a[244:247], v[124:127], v[80:83]\n"
        "v_lshlrev_b32 v58, 16, v192\n"
        "v_and_b32 v47, 0xffff0000, v180\n"
        "v_mfma_f32_16x16x32_bf16 v[84:87], a[240:243], v[128:131], v[84:87]\n"
        "ds_read_b128 v[120:123], v2 offset:3072\n"
        "v_and_b32 v51, 0xffff0000, v184\n"
        "v_mfma_f32_16x16x32_bf16 v[88:91], a[244:247], v[128:131], v[88:91]\n"
        "v_and_b32 v55, 0xffff0000, v188\n"
        "v_and_b32 v59, 0xffff0000, v192\n"
        "s_waitcnt lgkmcnt(4)\n"
        "v_mfma_f32_16x16x32_bf16 v[92:95], a[240:243], v[132:135], v[92:95]\n"
        "ds_read_b128 v[124:127], v2 offset:4096\n"
        "v_mov_b32 v181, v180\n"
        "v_mfma_f32_16x16x32_bf16 v[96:99], a[244:247], v[132:135], v[96:99]\n"
        "v_mov_b32 v185, v184\n"
        "v_mov_b32 v189, v188\n"
        "v_mfma_f32_16x16x32_bf16 v[100:103], a[240:243], v[136:139], v[100:103]\n"
        "ds_read_b128 v[128:131], v2 offset:5120\n"
        "v_mov_b32 v193, v192\n"
        "v_mfma_f32_16x16x32_bf16 v[104:107], a[244:247], v[136:139], v[104:107]\n"
        "v_sub_f32 v46, v44, v46\n"
        "v_sub_f32 v50, v48, v50\n"
        "v_sub_f32 v54, v52, v54\n"
        "v_sub_f32 v58, v56, v58\n"
        "v_sub_f32 v47, v45, v47\n"
        "v_sub_f32 v51, v49, v51\n"
        "v_sub_f32 v55, v53, v55\n"
        "v_sub_f32 v59, v57, v59\n"
        "v_cvt_pk_bf16_f32 v182, v46, v47\n"
        "v_cvt_pk_bf16_f32 v186, v50, v51\n"
        "v_cvt_pk_bf16_f32 v190, v54, v55\n"
        "v_cvt_pk_bf16_f32 v194, v58, v59\n"
        "s_add_u32 m0, s75, 0x3000\n"
        "s_add_u32 s77, s77, 0x1000\n"
        "buffer_load_dwordx4 v28, s[24:27], s77 offen lds\n"
        "v_mfma_f32_16x16x32_bf16 v[76:79], v[180:183], v[196:199], v[76:79]\n"
        "s_add_u32 m0, s75, 0x3000\n"
        "s_nop 0\n"
        "buffer_load_dwordx4 v29, s[24:27], s77 offen offset:1024 lds\n"
        "v_mfma_f32_16x16x32_bf16 v[80:83], v[184:187], v[196:199], v[80:83]\n"
        "v_mfma_f32_16x16x32_bf16 v[84:87], v[180:183], v[200:203], v[84:87]\n"
        "v_mfma_f32_16x16x32_bf16 v[88:91], v[184:187], v[200:203], v[88:91]\n"
        "v_mfma_f32_16x16x32_bf16 v[92:95], v[180:183], v[204:207], v[92:95]\n"
        "s_mul_i32 s77, s83, 0x8\n"
        "s_add_u32 s77, s77, s80\n"
        "v_mfma_f32_16x16x32_bf16 v[96:99], v[184:187], v[204:207], v[96:99]\n"
        "s_add_u32 m0, s75, 0x3800\n"
        "s_add_u32 s77, s77, 0x0\n"
        "v_mfma_f32_16x16x32_bf16 v[100:103], v[180:183], v[208:211], v[100:103]\n"
        "buffer_load_dwordx4 v28, s[24:27], s77 offen lds\n"
        "v_mfma_f32_16x16x32_bf16 v[104:107], v[184:187], v[208:211], v[104:107]\n"
        "s_waitcnt lgkmcnt(4)\n"
        "v_mfma_f32_16x16x32_bf16 v[44:47], a[8:11], v[108:111], 0\n"
        "ds_read_b128 v[132:135], v2 offset:6144\n"
        "s_add_u32 m0, s75, 0x3800\n"
        "v_mfma_f32_16x16x32_bf16 v[48:51], a[12:15], v[108:111], 0\n"
        "buffer_load_dwordx4 v29, s[24:27], s77 offen offset:1024 lds\n"
        "v_mfma_f32_16x16x32_bf16 v[52:55], a[8:11], v[112:115], 0\n"
        "ds_read_b128 v[136:139], v2 offset:7168\n"
        "v_mfma_f32_16x16x32_bf16 v[56:59], a[12:15], v[112:115], 0\n"
        "s_waitcnt lgkmcnt(4)\n"
        "v_mfma_f32_16x16x32_bf16 v[60:63], a[8:11], v[116:119], 0\n"
        "ds_read_b128 v[108:111], v2 offset:8192\n"
        "v_cvt_pk_bf16_f32 v244, v76, v77\n"
        "v_mfma_f32_16x16x32_bf16 v[64:67], a[12:15], v[116:119], 0\n"
        "v_cvt_pk_bf16_f32 v245, v78, v79\n"
        "v_cvt_pk_bf16_f32 v246, v80, v81\n"
        "v_mfma_f32_16x16x32_bf16 v[68:71], a[8:11], v[120:123], 0\n"
        "ds_read_b128 v[112:115], v2 offset:9216\n"
        "v_cvt_pk_bf16_f32 v247, v82, v83\n"
        "v_mfma_f32_16x16x32_bf16 v[72:75], a[12:15], v[120:123], 0\n"
        "v_pk_max_i16 v244, v244, 0\n"
        "v_pk_max_i16 v245, v245, 0\n"
        "s_waitcnt lgkmcnt(4)\n"
        "v_mfma_f32_16x16x32_bf16 v[44:47], a[24:27], v[124:127], v[44:47]\n"
        "ds_read_b128 v[116:119], v2 offset:10240\n"
        "v_pk_max_i16 v246, v246, 0\n"
        "v_mfma_f32_16x16x32_bf16 v[48:51], a[28:31], v[124:127], v[48:51]\n"
        "v_pk_max_i16 v247, v247, 0\n"
        "buffer_store_dwordx4 v[244:247], v5, s[44:47], s78 offen sc1\n"
        "v_mfma_f32_16x16x32_bf16 v[52:55], a[24:27], v[128:131], v[52:55]\n"
        "ds_read_b128 v[120:123], v2 offset:11264\n"
        "v_cvt_pk_bf16_f32 v248, v84, v85\n"
        "v_mfma_f32_16x16x32_bf16 v[56:59], a[28:31], v[128:131], v[56:59]\n"
        "v_cvt_pk_bf16_f32 v249, v86, v87\n"
        "v_cvt_pk_bf16_f32 v250, v88, v89\n"
        "s_waitcnt lgkmcnt(4)\n"
        "v_mfma_f32_16x16x32_bf16 v[60:63], a[24:27], v[132:135], v[60:63]\n"
        "ds_read_b128 v[124:127], v2 offset:12288\n"
        "v_cvt_pk_bf16_f32 v251, v90, v91\n"
        "v_mfma_f32_16x16x32_bf16 v[64:67], a[28:31], v[132:135], v[64:67]\n"
        "v_pk_max_i16 v248, v248, 0\n"
        "v_pk_max_i16 v249, v249, 0\n"
        "v_mfma_f32_16x16x32_bf16 v[68:71], a[24:27], v[136:139], v[68:71]\n"
        "ds_read_b128 v[128:131], v2 offset:13312\n"
        "v_pk_max_i16 v250, v250, 0\n"
        "v_mfma_f32_16x16x32_bf16 v[72:75], a[28:31], v[136:139], v[72:75]\n"
        "v_pk_max_i16 v251, v251, 0\n"
        "buffer_store_dwordx4 v[248:251], v5, s[44:47], s78 offen offset:2048 sc1\n"
        "s_waitcnt lgkmcnt(4)\n"
        "v_mfma_f32_16x16x32_bf16 v[44:47], a[40:43], v[108:111], v[44:47]\n"
        "ds_read_b128 v[132:135], v2 offset:14336\n"
        "s_addk_i32 s78, 4096\n"
        "v_mfma_f32_16x16x32_bf16 v[48:51], a[44:47], v[108:111], v[48:51]\n"
        "v_cvt_pk_bf16_f32 v244, v92, v93\n"
        "v_cvt_pk_bf16_f32 v245, v94, v95\n"
        "v_mfma_f32_16x16x32_bf16 v[52:55], a[40:43], v[112:115], v[52:55]\n"
        "ds_read_b128 v[136:139], v2 offset:15360\n"
        "v_cvt_pk_bf16_f32 v246, v96, v97\n"
        "v_mfma_f32_16x16x32_bf16 v[56:59], a[44:47], v[112:115], v[56:59]\n"
        "v_cvt_pk_bf16_f32 v247, v98, v99\n"
        "v_pk_max_i16 v244, v244, 0\n"
        "s_waitcnt lgkmcnt(4)\n"
        "v_mfma_f32_16x16x32_bf16 v[60:63], a[40:43], v[116:119], v[60:63]\n"
        "ds_read_b128 v[108:111], v2 offset:16384\n"
        "v_pk_max_i16 v245, v245, 0\n"
        "v_mfma_f32_16x16x32_bf16 v[64:67], a[44:47], v[116:119], v[64:67]\n"
        "v_pk_max_i16 v246, v246, 0\n"
        "v_pk_max_i16 v247, v247, 0\n"
        "v_mfma_f32_16x16x32_bf16 v[68:71], a[40:43], v[120:123], v[68:71]\n"
        "ds_read_b128 v[112:115], v2 offset:17408\n"
        "buffer_store_dwordx4 v[244:247], v5, s[44:47], s78 offen sc1\n"
        "v_mfma_f32_16x16x32_bf16 v[72:75], a[44:47], v[120:123], v[72:75]\n"
        "v_cvt_pk_bf16_f32 v248, v100, v101\n"
        "v_cvt_pk_bf16_f32 v249, v102, v103\n"
        "s_waitcnt lgkmcnt(4)\n"
        "v_mfma_f32_16x16x32_bf16 v[44:47], a[56:59], v[124:127], v[44:47]\n"
        "ds_read_b128 v[116:119], v2 offset:18432\n"
        "v_cvt_pk_bf16_f32 v250, v104, v105\n"
        "v_mfma_f32_16x16x32_bf16 v[48:51], a[60:63], v[124:127], v[48:51]\n"
        "v_cvt_pk_bf16_f32 v251, v106, v107\n"
        "v_pk_max_i16 v248, v248, 0\n"
        "v_mfma_f32_16x16x32_bf16 v[52:55], a[56:59], v[128:131], v[52:55]\n"
        "ds_read_b128 v[120:123], v2 offset:19456\n"
        "v_pk_max_i16 v249, v249, 0\n"
        "v_mfma_f32_16x16x32_bf16 v[56:59], a[60:63], v[128:131], v[56:59]\n"
        "v_pk_max_i16 v250, v250, 0\n"
        "v_pk_max_i16 v251, v251, 0\n"
        "s_waitcnt lgkmcnt(4)\n"
        "v_mfma_f32_16x16x32_bf16 v[60:63], a[56:59], v[132:135], v[60:63]\n"
        "ds_read_b128 v[124:127], v2 offset:20480\n"
        "buffer_store_dwordx4 v[248:251], v5, s[44:47], s78 offen offset:2048 sc1\n"
        "v_mfma_f32_16x16x32_bf16 v[64:67], a[60:63], v[132:135], v[64:67]\n"
        "s_add_u32 s78, s78, 0xd000\n"
        "s_add_u32 m0, s75, 0x0\n"
        "v_mfma_f32_16x16x32_bf16 v[68:71], a[56:59], v[136:139], v[68:71]\n"
        "ds_read_b128 v[128:131], v2 offset:21504\n"
        "s_add_u32 s77, s77, 0x1000\n"
        "v_mfma_f32_16x16x32_bf16 v[72:75], a[60:63], v[136:139], v[72:75]\n"
        "buffer_load_dwordx4 v28, s[24:27], s77 offen lds\n"
        "s_add_u32 m0, s75, 0x0\n"
        "s_waitcnt lgkmcnt(4)\n"
        "v_mfma_f32_16x16x32_bf16 v[44:47], a[72:75], v[108:111], v[44:47]\n"
        "ds_read_b128 v[132:135], v2 offset:22528\n"
        "buffer_load_dwordx4 v29, s[24:27], s77 offen offset:1024 lds\n"
        "v_mfma_f32_16x16x32_bf16 v[48:51], a[76:79], v[108:111], v[48:51]\n"
        "s_waitcnt vmcnt(22)\n"
        "ds_read_b64_tr_b16 a[0:1], v30 offset:2048\n"
        "v_mfma_f32_16x16x32_bf16 v[52:55], a[72:75], v[112:115], v[52:55]\n"
        "ds_read_b128 v[136:139], v2 offset:23552\n"
        "ds_read_b64_tr_b16 a[4:5], v30 offset:2056\n"
        "v_mfma_f32_16x16x32_bf16 v[56:59], a[76:79], v[112:115], v[56:59]\n"
        "ds_read_b64_tr_b16 a[16:17], v31 offset:2048\n"
        "ds_read_b64_tr_b16 a[20:21], v31 offset:2056\n"
        "s_waitcnt lgkmcnt(8)\n"
        "v_mfma_f32_16x16x32_bf16 v[60:63], a[72:75], v[116:119], v[60:63]\n"
        "s_branch .Lku5\n"
        ".Lkt5:\n"
        "s_setpc_b64 s[86:87]\n"
        ".Lku5:\n"
        "ds_read_b128 v[108:111], v2 offset:24576\n"
        "s_mul_i32 s77, s84, 0x8\n"
        "v_mfma_f32_16x16x32_bf16 v[64:67], a[76:79], v[116:119], v[64:67]\n"
        "s_add_u32 s77, s77, s80\n"
        "s_add_u32 m0, s75, 0x800\n"
        "v_mfma_f32_16x16x32_bf16 v[68:71], a[72:75], v[120:123], v[68:71]\n"
        "ds_read_b128 v[112:115], v2 offset:25600\n"
        "s_add_u32 s77, s77, 0x0\n"
        "v_mfma_f32_16x16x32_bf16 v[72:75], a[76:79], v[120:123], v[72:75]\n"
        "buffer_load_dwordx4 v28, s[24:27], s77 offen lds\n"
        "s_add_u32 m0, s75, 0x800\n"
        "s_waitcnt lgkmcnt(8)\n"
        "v_mfma_f32_16x16x32_bf16 v[44:47], a[88:91], v[124:127], v[44:47]\n"
        "ds_read_b128 v[116:119], v2 offset:26624\n"
        "buffer_load_dwordx4 v29, s[24:27], s77 offen offset:1024 lds\n"
        "v_mfma_f32_16x16x32_bf16 v[48:51], a[92:95], v[124:127], v[48:51]\n"
        "s_waitcnt vmcnt(22)\n"
        "ds_read_b64_tr_b16 a[2:3], v30 offset:4096\n"
        "v_mfma_f32_16x16x32_bf16 v[52:55], a[88:91], v[128:131], v[52:55]\n"
        "ds_read_b128 v[120:123], v2 offset:27648\n"
        "ds_read_b64_tr_b16 a[6:7], v30 offset:4104\n"
        "v_mfma_f32_16x16x32_bf16 v[56:59], a[92:95], v[128:131], v[56:59]\n"
        "ds_read_b64_tr_b16 a[18:19], v31 offset:4096\n"
        "ds_read_b64_tr_b16 a[22:23], v31 offset:4104\n"
        "s_waitcnt lgkmcnt(11)\n"
        "v_mfma_f32_16x16x32_bf16 v[60:63], a[88:91], v[132:135], v[60:63]\n"
        "ds_read_b128 v[124:127], v2 offset:28672\n"
        "s_waitcnt lgkmcnt(9)\n"
        "v_mfma_f32_16x16x32_bf16 v[64:67], a[92:95], v[132:135], v[64:67]\n"
        "s_add_u32 m0, s74, 0x800\n"
        "s_add_u32 s77, s77, 0x1000\n"
        "v_mfma_f32_16x16x32_bf16 v[68:71], a[88:91], v[136:139], v[68:71]\n"
        "ds_read_b128 v[128:131], v2 offset:29696\n"
        "buffer_load_dwordx4 v28, s[24:27], s77 offen lds\n"
        "v_mfma_f32_16x16x32_bf16 v[72:75], a[92:95], v[136:139], v[72:75]\n"
        "s_add_u32 m0, s74, 0x800\n"
        "s_nop 0\n"
        "buffer_load_dwordx4 v29, s[24:27], s77 offen offset:1024 lds\n"
        "s_waitcnt lgkmcnt(8)\n"
        "v_mfma_f32_16x16x32_bf16 v[44:47], a[104:107], v[108:111], v[44:47]\n"
        "ds_read_b128 v[132:135], v2 offset:30720\n"
        "s_waitcnt vmcnt(22)\n"
        "v_mfma_f32_16x16x32_bf16 v[48:51], a[108:111], v[108:111], v[48:51]\n"
        "ds_read_b64_tr_b16 a[32:33], v30\n"
        "ds_read_b64_tr_b16 a[36:37], v30 offset:8\n"
        "v_mfma_f32_16x16x32_bf16 v[52:55], a[104:107], v[112:115], v[52:55]\n"
        "ds_read_b128 v[136:139], v2 offset:31744\n"
        "ds_read_b64_tr_b16 a[48:49], v31\n"
        "v_mfma_f32_16x16x32_bf16 v[56:59], a[108:111], v[112:115], v[56:59]\n"
        "ds_read_b64_tr_b16 a[52:53], v31 offset:8\n"
        "s_waitcnt lgkmcnt(8)\n"
        "v_mfma_f32_16x16x32_bf16 v[60:63], a[104:107], v[116:119], v[60:63]\n"
        "ds_read_b128 v[108:111], v2 offset:32768\n"
        "s_mul_i32 s77, s85, 0x8\n"
        "v_mfma_f32_16x16x32_bf16 v[64:67], a[108:111], v[116:119], v[64:67]\n"
        "s_add_u32 s77, s77, s80\n"
        "s_add_u32 m0, s74, 0x1000\n"
        "v_mfma_f32_16x16x32_bf16 v[68:71], a[104:107], v[120:123], v[68:71]\n"
        "ds_read_b128 v[112:115], v2 offset:33792\n"
        "s_add_u32 s77, s77, 0x0\n"
        "v_mfma_f32_16x16x32_bf16 v[72:75], a[108:111], v[120:123], v[72:75]\n"
        "buffer_load_dwordx4 v28, s[24:27], s77 offen lds\n"
        "s_add_u32 m0, s74, 0x1000\n"
        "s_waitcnt lgkmcnt(8)\n"
        "v_mfma_f32_16x16x32_bf16 v[44:47], a[120:123], v[124:127], v[44:47]\n"
        "ds_read_b128 v[116:119], v2 offset:34816\n"
        "buffer_load_dwordx4 v29, s[24:27], s77 offen offset:1024 lds\n"
        "v_mfma_f32_16x16x32_bf16 v[48:51], a[124:127], v[124:127], v[48:51]\n"
        "s_waitcnt vmcnt(22)\n"
        "ds_read_b64_tr_b16 a[34:35], v32 offset:4096\n"
        "v_mfma_f32_16x16x32_bf16 v[52:55], a[120:123], v[128:131], v[52:55]\n"
        "ds_read_b128 v[120:123], v2 offset:35840\n"
        "ds_read_b64_tr_b16 a[38:39], v32 offset:4104\n"
        "v_mfma_f32_16x16x32_bf16 v[56:59], a[124:127], v[128:131], v[56:59]\n"
        "ds_read_b64_tr_b16 a[50:51], v33 offset:4096\n"
        "ds_read_b64_tr_b16 a[54:55], v33 offset:4104\n"
        "s_waitcnt lgkmcnt(10)\n"
        "v_mfma_f32_16x16x32_bf16 v[60:63], a[120:123], v[132:135], v[60:63]\n"
        "ds_read_b128 v[124:127], v2 offset:36864\n"
        "s_waitcnt lgkmcnt(9)\n"
        "v_mfma_f32_16x16x32_bf16 v[64:67], a[124:127], v[132:135], v[64:67]\n"
        "s_add_u32 m0, s74, 0x0\n"
        "s_add_u32 s77, s77, 0x1000\n"
        "v_mfma_f32_16x16x32_bf16 v[68:71], a[120:123], v[136:139], v[68:71]\n"
        "ds_read_b128 v[128:131], v2 offset:37888\n"
        "buffer_load_dwordx4 v28, s[24:27], s77 offen lds\n"
        "v_mfma_f32_16x16x32_bf16 v[72:75], a[124:127], v[136:139], v[72:75]\n"
        "s_add_u32 m0, s74, 0x0\n"
        "s_nop 0\n"
        "buffer_load_dwordx4 v29, s[24:27], s77 offen offset:1024 lds\n"
        "s_waitcnt lgkmcnt(8)\n"
        "v_mfma_f32_16x16x32_bf16 v[44:47], a[136:139], v[108:111], v[44:47]\n"
        "ds_read_b128 v[132:135], v2 offset:38912\n"
        "s_waitcnt vmcnt(22)\n"
        "v_mfma_f32_16x16x32_bf16 v[48:51], a[140:143], v[108:111], v[48:51]\n"
        "ds_read_b64_tr_b16 a[64:65], v32 offset:6144\n"
        "ds_read_b64_tr_b16 a[68:69], v32 offset:6152\n"
        "v_mfma_f32_16x16x32_bf16 v[52:55], a[136:139], v[112:115], v[52:55]\n"
        "ds_read_b128 v[136:139], v2 offset:39936\n"
        "ds_read_b64_tr_b16 a[80:81], v33 offset:6144\n"
        "v_mfma_f32_16x16x32_bf16 v[56:59], a[140:143], v[112:115], v[56:59]\n"
        "ds_read_b64_tr_b16 a[84:85], v33 offset:6152\n"
        "s_waitcnt lgkmcnt(8)\n"
        "v_mfma_f32_16x16x32_bf16 v[60:63], a[136:139], v[116:119], v[60:63]\n"
        "ds_read_b128 v[108:111], v2 offset:40960\n"
        "s_mul_i32 s77, s60, 0x8\n"
        "v_mfma_f32_16x16x32_bf16 v[64:67], a[140:143], v[116:119], v[64:67]\n"
        "s_add_u32 s77, s77, s80\n"
        "s_add_u32 m0, s75, 0x1000\n"
        "v_mfma_f32_16x16x32_bf16 v[68:71], a[136:139], v[120:123], v[68:71]\n"
        "ds_read_b128 v[112:115], v2 offset:41984\n"
        "s_add_u32 s77, s77, 0x8000\n"
        "v_mfma_f32_16x16x32_bf16 v[72:75], a[140:143], v[120:123], v[72:75]\n"
        "buffer_load_dwordx4 v28, s[24:27], s77 offen lds\n"
        "s_add_u32 m0, s75, 0x1000\n"
        "s_waitcnt lgkmcnt(8)\n"
        "v_mfma_f32_16x16x32_bf16 v[44:47], a[152:155], v[124:127], v[44:47]\n"
        "ds_read_b128 v[116:119], v2 offset:43008\n"
        "buffer_load_dwordx4 v29, s[24:27], s77 offen offset:1024 lds\n"
        "v_mfma_f32_16x16x32_bf16 v[48:51], a[156:159], v[124:127], v[48:51]\n"
        "s_waitcnt vmcnt(22)\n"
        "ds_read_b64_tr_b16 a[66:67], v32 offset:8192\n"
        "v_mfma_f32_16x16x32_bf16 v[52:55], a[152:155], v[128:131], v[52:55]\n"
        "ds_read_b128 v[120:123], v2 offset:44032\n"
        "ds_read_b64_tr_b16 a[70:71], v32 offset:8200\n"
        "v_mfma_f32_16x16x32_bf16 v[56:59], a[156:159], v[128:131], v[56:59]\n"
        "ds_read_b64_tr_b16 a[82:83], v33 offset:8192\n"
        "ds_read_b64_tr_b16 a[86:87], v33 offset:8200\n"
        "s_waitcnt lgkmcnt(10)\n"
        "v_mfma_f32_16x16x32_bf16 v[60:63], a[152:155], v[132:135], v[60:63]\n"
        "ds_read_b128 v[124:127], v2 offset:45056\n"
        "s_waitcnt lgkmcnt(9)\n"
        "v_mfma_f32_16x16x32_bf16 v[64:67], a[156:159], v[132:135], v[64:67]\n"
        "s_add_u32 m0, s75, 0x1800\n"
        "s_add_u32 s77, s77, 0x1000\n"
        "v_mfma_f32_16x16x32_bf16 v[68:71], a[152:155], v[136:139], v[68:71]\n"
        "ds_read_b128 v[128:131], v2 offset:46080\n"
        "buffer_load_dwordx4 v28, s[24:27], s77 offen lds\n"
        "v_mfma_f32_16x16x32_bf16 v[72:75], a[156:159], v[136:139], v[72:75]\n"
        "s_add_u32 m0, s75, 0x1800\n"
        "s_nop 0\n"
        "buffer_load_dwordx4 v29, s[24:27], s77 offen offset:1024 lds\n"
        "s_waitcnt lgkmcnt(8)\n"
        "v_mfma_f32_16x16x32_bf16 v[44:47], a[168:171], v[108:111], v[44:47]\n"
        "ds_read_b128 v[132:135], v2 offset:47104\n"
        "s_waitcnt vmcnt(22)\n"
        "v_mfma_f32_16x16x32_bf16 v[48:51], a[172:175], v[108:111], v[48:51]\n"
        "ds_read_b64_tr_b16 a[96:97], v32 offset:10240\n"
        "ds_read_b64_tr_b16 a[100:101], v32 offset:10248\n"
        "v_mfma_f32_16x16x32_bf16 v[52:55], a[168:171], v[112:115], v[52:55]\n"
        "ds_read_b128 v[136:139], v2 offset:48128\n"
        "ds_read_b64_tr_b16 a[112:113], v33 offset:10240\n"
        "v_mfma_f32_16x16x32_bf16 v[56:59], a[172:175], v[112:115], v[56:59]\n"
        "ds_read_b64_tr_b16 a[116:117], v33 offset:10248\n"
        "s_waitcnt lgkmcnt(8)\n"
        "v_mfma_f32_16x16x32_bf16 v[60:63], a[168:171], v[116:119], v[60:63]\n"
        "ds_read_b128 v[108:111], v2 offset:49152\n"
        "s_mul_i32 s77, s68, 0x8\n"
        "v_mfma_f32_16x16x32_bf16 v[64:67], a[172:175], v[116:119], v[64:67]\n"
        "s_add_u32 s77, s77, s80\n"
        "s_add_u32 m0, s75, 0x2000\n"
        "v_mfma_f32_16x16x32_bf16 v[68:71], a[168:171], v[120:123], v[68:71]\n"
        "ds_read_b128 v[112:115], v2 offset:50176\n"
        "s_add_u32 s77, s77, 0x8000\n"
        "v_mfma_f32_16x16x32_bf16 v[72:75], a[172:175], v[120:123], v[72:75]\n"
        "buffer_load_dwordx4 v28, s[24:27], s77 offen lds\n"
        "s_add_u32 m0, s75, 0x2000\n"
        "s_waitcnt lgkmcnt(8)\n"
        "v_mfma_f32_16x16x32_bf16 v[44:47], a[184:187], v[124:127], v[44:47]\n"
        "ds_read_b128 v[116:119], v2 offset:51200\n"
        "buffer_load_dwordx4 v29, s[24:27], s77 offen offset:1024 lds\n"
        "v_mfma_f32_16x16x32_bf16 v[48:51], a[188:191], v[124:127], v[48:51]\n"
        "s_waitcnt vmcnt(16)\n"
        "s_add_u32 s59, s64, 0x2000\n"
        "v_mfma_f32_16x16x32_bf16 v[52:55], a[184:187], v[128:131], v[52:55]\n"
        "ds_read_b128 v[120:123], v2 offset:52224\n"
        "buffer_store_dwordx4 v[252:255], v19, s[40:43], s59 offen sc1\n"
        "v_mfma_f32_16x16x32_bf16 v[56:59], a[188:191], v[128:131], v[56:59]\n"
        "s_waitcnt lgkmcnt(4)\n"
        "s_add_u32 m0, s75, 0x2800\n"
        "v_mfma_f32_16x16x32_bf16 v[60:63], a[184:187], v[132:135], v[60:63]\n"
        "ds_read_b128 v[124:127], v2 offset:53248\n"
        "s_add_u32 s77, s77, 0x1000\n"
        "v_mfma_f32_16x16x32_bf16 v[64:67], a[188:191], v[132:135], v[64:67]\n"
        "buffer_load_dwordx4 v28, s[24:27], s77 offen lds\n"
        "ds_read_b64_tr_b16 a[98:99], v32 offset:12288\n"
        "v_mfma_f32_16x16x32_bf16 v[68:71], a[184:187], v[136:139], v[68:71]\n"
        "ds_read_b128 v[128:131], v2 offset:54272\n"
        "ds_read_b64_tr_b16 a[102:103], v32 offset:12296\n"
        "v_mfma_f32_16x16x32_bf16 v[72:75], a[188:191], v[136:139], v[72:75]\n"
        "ds_read_b64_tr_b16 a[114:115], v33 offset:12288\n"
        "ds_read_b64_tr_b16 a[118:119], v33 offset:12296\n"
        "s_waitcnt lgkmcnt(8)\n"
        "v_mfma_f32_16x16x32_bf16 v[44:47], a[200:203], v[108:111], v[44:47]\n"
        "ds_read_b128 v[132:135], v2 offset:55296\n"
        "s_add_u32 m0, s75, 0x2800\n"
        "v_mfma_f32_16x16x32_bf16 v[48:51], a[204:207], v[108:111], v[48:51]\n"
        "buffer_load_dwordx4 v29, s[24:27], s77 offen offset:1024 lds\n"
        "ds_read_b64_tr_b16 a[128:129], v32 offset:14336\n"
        "v_mfma_f32_16x16x32_bf16 v[52:55], a[200:203], v[112:115], v[52:55]\n"
        "ds_read_b128 v[136:139], v2 offset:56320\n"
        "ds_read_b64_tr_b16 a[132:133], v32 offset:14344\n"
        "v_mfma_f32_16x16x32_bf16 v[56:59], a[204:207], v[112:115], v[56:59]\n"
        "ds_read_b64_tr_b16 a[144:145], v33 offset:14336\n"
        "ds_read_b64_tr_b16 a[148:149], v33 offset:14344\n"
        "s_waitcnt lgkmcnt(12)\n"
        "v_mfma_f32_16x16x32_bf16 v[60:63], a[200:203], v[116:119], v[60:63]\n"
        "ds_read_b128 v[108:111], v2 offset:57344\n"
        "s_waitcnt lgkmcnt(7)\n"
        "v_mfma_f32_16x16x32_bf16 v[64:67], a[204:207], v[116:119], v[64:67]\n"
        "s_mul_i32 s77, s81, 0x8\n"
        "s_add_u32 s77, s77, s80\n"
        "v_mfma_f32_16x16x32_bf16 v[68:71], a[200:203], v[120:123], v[68:71]\n"
        "ds_read_b128 v[112:115], v2 offset:58368\n"
        "s_add_u32 m0, s75, 0x3000\n"
        "v_mfma_f32_16x16x32_bf16 v[72:75], a[204:207], v[120:123], v[72:75]\n"
        "s_add_u32 s77, s77, 0x8000\n"
        "buffer_load_dwordx4 v28, s[24:27], s77 offen lds\n"
        "v_mfma_f32_16x16x32_bf16 v[44:47], a[216:219], v[124:127], v[44:47]\n"
        "ds_read_b128 v[116:119], v2 offset:59392\n"
        "s_add_u32 m0, s75, 0x3000\n"
        "v_mfma_f32_16x16x32_bf16 v[48:51], a[220:223], v[124:127], v[48:51]\n"
        "buffer_load_dwordx4 v29, s[24:27], s77 offen offset:1024 lds\n"
        "s_waitcnt vmcnt(19)\n"
        "v_mfma_f32_16x16x32_bf16 v[52:55], a[216:219], v[128:131], v[52:55]\n"
        "ds_read_b128 v[120:123], v2 offset:60416\n"
        "ds_read_b64_tr_b16 a[130:131], v32\n"
        "v_mfma_f32_16x16x32_bf16 v[56:59], a[220:223], v[128:131], v[56:59]\n"
        "ds_read_b64_tr_b16 a[134:135], v32 offset:8\n"
        "ds_read_b64_tr_b16 a[146:147], v33\n"
        "s_waitcnt lgkmcnt(10)\n"
        "v_mfma_f32_16x16x32_bf16 v[60:63], a[216:219], v[132:135], v[60:63]\n"
        "ds_read_b128 v[124:127], v2 offset:61440\n"
        "ds_read_b64_tr_b16 a[150:151], v33 offset:8\n"
        "v_mfma_f32_16x16x32_bf16 v[64:67], a[220:223], v[132:135], v[64:67]\n"
        "s_waitcnt lgkmcnt(9)\n"
        "s_add_u32 m0, s75, 0x3800\n"
        "v_mfma_f32_16x16x32_bf16 v[68:71], a[216:219], v[136:139], v[68:71]\n"
        "ds_read_b128 v[128:131], v2 offset:62464\n"
        "s_add_u32 s77, s77, 0x1000\n"
        "v_mfma_f32_16x16x32_bf16 v[72:75], a[220:223], v[136:139], v[72:75]\n"
        "buffer_load_dwordx4 v28, s[24:27], s77 offen lds\n"
        "s_add_u32 m0, s75, 0x3800\n"
        "s_waitcnt lgkmcnt(8)\n"
        "v_mfma_f32_16x16x32_bf16 v[44:47], a[232:235], v[108:111], v[44:47]\n"
        "ds_read_b128 v[132:135], v2 offset:63488\n"
        "buffer_load_dwordx4 v29, s[24:27], s77 offen offset:1024 lds\n"
        "v_mfma_f32_16x16x32_bf16 v[48:51], a[236:239], v[108:111], v[48:51]\n"
        "s_waitcnt vmcnt(19)\n"
        "ds_read_b64_tr_b16 a[160:161], v32 offset:2048\n"
        "v_mfma_f32_16x16x32_bf16 v[52:55], a[232:235], v[112:115], v[52:55]\n"
        "ds_read_b128 v[136:139], v2 offset:64512\n"
        "ds_read_b64_tr_b16 a[164:165], v32 offset:2056\n"
        "v_mfma_f32_16x16x32_bf16 v[56:59], a[236:239], v[112:115], v[56:59]\n"
        "ds_read_b64_tr_b16 a[176:177], v33 offset:2048\n"
        "ds_read_b64_tr_b16 a[180:181], v33 offset:2056\n"
        "s_waitcnt lgkmcnt(12)\n"
        "v_mfma_f32_16x16x32_bf16 v[60:63], a[232:235], v[116:119], v[60:63]\n"
        "s_waitcnt lgkmcnt(7)\n"
        "s_mul_i32 s77, s82, 0x8\n"
        "v_mfma_f32_16x16x32_bf16 v[64:67], a[236:239], v[116:119], v[64:67]\n"
        "s_add_u32 s77, s77, s80\n"
        "s_add_u32 m0, s75, 0x0\n"
        "v_mfma_f32_16x16x32_bf16 v[68:71], a[232:235], v[120:123], v[68:71]\n"
        "s_add_u32 s77, s77, 0x8000\n"
        "buffer_load_dwordx4 v28, s[24:27], s77 offen lds\n"
        "v_mfma_f32_16x16x32_bf16 v[72:75], a[236:239], v[120:123], v[72:75]\n"
        "s_add_u32 m0, s75, 0x0\n"
        "s_nop 0\n"
        "buffer_load_dwordx4 v29, s[24:27], s77 offen offset:1024 lds\n"
        "s_waitcnt lgkmcnt(6)\n"
        "v_mfma_f32_16x16x32_bf16 v[44:47], a[248:251], v[124:127], v[44:47]\n"
        "s_waitcnt vmcnt(19)\n"
        "ds_read_b64_tr_b16 a[162:163], v30 offset:2048\n"
        "v_mfma_f32_16x16x32_bf16 v[48:51], a[252:255], v[124:127], v[48:51]\n"
        "ds_read_b64_tr_b16 a[166:167], v30 offset:2056\n"
        "ds_read_b64_tr_b16 a[178:179], v31 offset:2048\n"
        "v_mfma_f32_16x16x32_bf16 v[52:55], a[248:251], v[128:131], v[52:55]\n"
        "ds_read_b64_tr_b16 a[182:183], v31 offset:2056\n"
        "s_waitcnt vmcnt(17)\n"
        "v_mfma_f32_16x16x32_bf16 v[56:59], a[252:255], v[128:131], v[56:59]\n"
        "ds_read_b64_tr_b16 a[192:193], v30 offset:4096\n"
        "ds_read_b64_tr_b16 a[196:197], v30 offset:4104\n"
        "ds_read_b64_tr_b16 a[208:209], v31 offset:4096\n"
        "ds_read_b64_tr_b16 a[212:213], v31 offset:4104\n"
        "s_waitcnt lgkmcnt(0)\n"
        "s_barrier\n"
        "v_mfma_f32_16x16x32_bf16 v[60:63], a[248:251], v[132:135], v[60:63]\n"
        "s_waitcnt vmcnt(15)\n"
        "ds_read_b64_tr_b16 a[194:195], v30\n"
        "v_mfma_f32_16x16x32_bf16 v[64:67], a[252:255], v[132:135], v[64:67]\n"
        "ds_read_b64_tr_b16 a[198:199], v30 offset:8\n"
        "ds_read_b64_tr_b16 a[210:211], v31\n"
        "v_mfma_f32_16x16x32_bf16 v[68:71], a[248:251], v[136:139], v[68:71]\n"
        "ds_read_b64_tr_b16 a[214:215], v31 offset:8\n"
        "v_mfma_f32_16x16x32_bf16 v[72:75], a[252:255], v[136:139], v[72:75]\n"
        "v_mfma_f32_16x16x32_bf16 v[44:47], v[188:191], v[196:199], v[44:47]\n"
        "v_mfma_f32_16x16x32_bf16 v[48:51], v[192:195], v[196:199], v[48:51]\n"
        "v_mfma_f32_16x16x32_bf16 v[52:55], v[188:191], v[200:203], v[52:55]\n"
        "v_mfma_f32_16x16x32_bf16 v[56:59], v[192:195], v[200:203], v[56:59]\n"
        "v_mfma_f32_16x16x32_bf16 v[60:63], v[188:191], v[204:207], v[60:63]\n"
        "v_mfma_f32_16x16x32_bf16 v[64:67], v[192:195], v[204:207], v[64:67]\n"
        "v_mfma_f32_16x16x32_bf16 v[68:71], v[188:191], v[208:211], v[68:71]\n"
        "v_mfma_f32_16x16x32_bf16 v[72:75], v[192:195], v[208:211], v[72:75]\n"
        "s_nop 0\n"
        "v_cvt_pk_bf16_f32 v244, v44, v45\n"
        "v_cvt_pk_bf16_f32 v245, v46, v47\n"
        "v_cvt_pk_bf16_f32 v246, v48, v49\n"
        "v_cvt_pk_bf16_f32 v247, v50, v51\n"
        "v_pk_max_i16 v244, v244, 0\n"
        "v_pk_max_i16 v245, v245, 0\n"
        "v_pk_max_i16 v246, v246, 0\n"
        "v_pk_max_i16 v247, v247, 0\n"
        "buffer_store_dwordx4 v[244:247], v5, s[44:47], s78 offen sc1\n"
        "v_cvt_pk_bf16_f32 v248, v52, v53\n"
        "v_cvt_pk_bf16_f32 v249, v54, v55\n"
        "v_cvt_pk_bf16_f32 v250, v56, v57\n"
        "v_cvt_pk_bf16_f32 v251, v58, v59\n"
        "v_pk_max_i16 v248, v248, 0\n"
        "v_pk_max_i16 v249, v249, 0\n"
        "v_pk_max_i16 v250, v250, 0\n"
        "v_pk_max_i16 v251, v251, 0\n"
        "buffer_store_dwordx4 v[248:251], v5, s[44:47], s78 offen offset:2048 sc1\n"
        "s_addk_i32 s78, 4096\n"
        "v_cvt_pk_bf16_f32 v244, v60, v61\n"
        "v_cvt_pk_bf16_f32 v245, v62, v63\n"
        "v_cvt_pk_bf16_f32 v246, v64, v65\n"
        "v_cvt_pk_bf16_f32 v247, v66, v67\n"
        "v_pk_max_i16 v244, v244, 0\n"
        "v_pk_max_i16 v245, v245, 0\n"
        "v_pk_max_i16 v246, v246, 0\n"
        "v_pk_max_i16 v247, v247, 0\n"
        "buffer_store_dwordx4 v[244:247], v5, s[44:47], s78 offen sc1\n"
        "v_cvt_pk_bf16_f32 v248, v68, v69\n"
        "v_cvt_pk_bf16_f32 v249, v70, v71\n"
        "v_cvt_pk_bf16_f32 v250, v72, v73\n"
        "v_cvt_pk_bf16_f32 v251, v74, v75\n"
        "v_pk_max_i16 v248, v248, 0\n"
        "v_pk_max_i16 v249, v249, 0\n"
        "v_pk_max_i16 v250, v250, 0\n"
        "v_pk_max_i16 v251, v251, 0\n"
        "buffer_store_dwordx4 v[248:251], v5, s[44:47], s78 offen offset:2048 sc1\n"
        "s_add_u32 s78, s78, 0xd000\n"
        "s_waitcnt vmcnt(17)\n"
        "ds_read_b64_tr_b16 a[8:9], v32 offset:4096\n"
        "ds_read_b64_tr_b16 a[12:13], v32 offset:4104\n"
        "ds_read_b64_tr_b16 a[24:25], v33 offset:4096\n"
        "ds_read_b64_tr_b16 a[28:29], v33 offset:4104\n"
        "s_waitcnt vmcnt(15)\n"
        "ds_read_b64_tr_b16 a[10:11], v32 offset:6144\n"
        "ds_read_b64_tr_b16 a[14:15], v32 offset:6152\n"
        "ds_read_b64_tr_b16 a[26:27], v33 offset:6144\n"
        "ds_read_b64_tr_b16 a[30:31], v33 offset:6152\n"
        "s_add_u32 s28, s69, 0x2000\n"
        "buffer_load_dwordx2 v[20:21], v11, s[40:43], s28 offen sc1\n"
        "s_add_u32 m0, s75, 0x800\n"
        "s_add_u32 s77, s77, 0x1000\n"
        "buffer_load_dwordx4 v28, s[24:27], s77 offen lds\n"
        "buffer_load_dwordx4 v29, s[24:27], s77 offen offset:1024 lds\n"
        "s_mul_i32 s77, s83, 0x8\n"
        "s_add_u32 s77, s77, s80\n"
        "s_add_u32 m0, s74, 0x800\n"
        "s_add_u32 s77, s77, 0x8000\n"
        "buffer_load_dwordx4 v28, s[24:27], s77 offen lds\n"
        "buffer_load_dwordx4 v29, s[24:27], s77 offen offset:1024 lds\n"
        "s_waitcnt vmcnt(18)\n"
        "ds_read_b64_tr_b16 a[40:41], v32 offset:8192\n"
        "ds_read_b64_tr_b16 a[44:45], v32 offset:8200\n"
        "ds_read_b64_tr_b16 a[56:57], v33 offset:8192\n"
        "ds_read_b64_tr_b16 a[60:61], v33 offset:8200\n"
        "s_waitcnt vmcnt(15)\n"
        "ds_read_b64_tr_b16 a[42:43], v32 offset:10240\n"
        "ds_read_b64_tr_b16 a[46:47], v32 offset:10248\n"
        "ds_read_b64_tr_b16 a[58:59], v33 offset:10240\n"
        "ds_read_b64_tr_b16 a[62:63], v33 offset:10248\n"
        "s_waitcnt vmcnt(13)\n"
        "ds_read_b64_tr_b16 a[72:73], v32 offset:12288\n"
        "ds_read_b64_tr_b16 a[76:77], v32 offset:12296\n"
        "ds_read_b64_tr_b16 a[88:89], v33 offset:12288\n"
        "ds_read_b64_tr_b16 a[92:93], v33 offset:12296\n"
        "s_waitcnt vmcnt(11)\n"
        "ds_read_b64_tr_b16 a[74:75], v32 offset:14336\n"
        "ds_read_b64_tr_b16 a[78:79], v32 offset:14344\n"
        "ds_read_b64_tr_b16 a[90:91], v33 offset:14336\n"
        "ds_read_b64_tr_b16 a[94:95], v33 offset:14344\n"
        "s_waitcnt vmcnt(9)\n"
        "ds_read_b64_tr_b16 a[104:105], v32\n"
        "ds_read_b64_tr_b16 a[108:109], v32 offset:8\n"
        "ds_read_b64_tr_b16 a[120:121], v33\n"
        "ds_read_b64_tr_b16 a[124:125], v33 offset:8\n"
        "s_waitcnt vmcnt(4)\n"
        "v_cmp_ne_u64 vcc, s[56:57], v[20:21]\n"
        "s_nop 0\n"
        "s_cbranch_vccnz .Lkp5\n"
        ".Lkq5:\n"
        "s_add_u32 s59, s64, 0x2800\n"
        "buffer_store_dwordx4 v[252:255], v19, s[40:43], s59 offen sc1\n"
        "s_add_u32 s70, s60, s79\n"
        "buffer_load_dwordx4 v[148:151], v12, s[40:43], s70 offen sc1\n"
        "buffer_load_dwordx4 v[152:155], v12, s[40:43], s70 offen offset:2048 sc1\n"
        "buffer_load_dwordx4 v[156:159], v15, s[40:43], s70 offen sc1\n"
        "buffer_load_dwordx4 v[160:163], v15, s[40:43], s70 offen offset:2048 sc1\n"
        "s_add_u32 s70, s68, s79\n"
        "buffer_load_dwordx4 v[164:167], v12, s[40:43], s70 offen sc1\n"
        "buffer_load_dwordx4 v[168:171], v12, s[40:43], s70 offen offset:2048 sc1\n"
        "buffer_load_dwordx4 v[172:175], v15, s[40:43], s70 offen sc1\n"
        "buffer_load_dwordx4 v[176:179], v15, s[40:43], s70 offen offset:2048 sc1\n"
        "s_waitcnt vmcnt(7)\n"
        "v_mfma_f32_16x16x32_bf16 v[76:79], a[0:3], v[148:151], 0\n"
        "ds_read_b64_tr_b16 a[106:107], v32 offset:2048\n"
        "ds_read_b64_tr_b16 a[110:111], v32 offset:2056\n"
        "v_mfma_f32_16x16x32_bf16 v[80:83], a[4:7], v[148:151], 0\n"
        "ds_read_b64_tr_b16 a[122:123], v33 offset:2048\n"
        "ds_read_b64_tr_b16 a[126:127], v33 offset:2056\n"
        "v_mfma_f32_16x16x32_bf16 v[108:111], a[16:19], v[148:151], 0\n"
        "s_add_u32 s70, s81, s79\n"
        "buffer_load_dwordx4 v[180:183], v12, s[40:43], s70 offen sc1\n"
        "v_mfma_f32_16x16x32_bf16 v[112:115], a[20:23], v[148:151], 0\n"
        "s_add_u32 m0, s74, 0x1000\n"
        "s_add_u32 s77, s77, 0x1000\n"
        "s_waitcnt vmcnt(7)\n"
        "v_mfma_f32_16x16x32_bf16 v[84:87], a[0:3], v[152:155], 0\n"
        "buffer_load_dwordx4 v28, s[24:27], s77 offen lds\n"
        "buffer_load_dwordx4 v[184:187], v12, s[40:43], s70 offen offset:2048 sc1\n"
        "v_mfma_f32_16x16x32_bf16 v[88:91], a[4:7], v[152:155], 0\n"
        "ds_read_b64_tr_b16 a[136:137], v30 offset:2048\n"
        "ds_read_b64_tr_b16 a[140:141], v30 offset:2056\n"
        "v_mfma_f32_16x16x32_bf16 v[116:119], a[16:19], v[152:155], 0\n"
        "ds_read_b64_tr_b16 a[152:153], v31 offset:2048\n"
        "ds_read_b64_tr_b16 a[156:157], v31 offset:2056\n"
        "v_mfma_f32_16x16x32_bf16 v[120:123], a[20:23], v[152:155], 0\n"
        "buffer_load_dwordx4 v[188:191], v15, s[40:43], s70 offen sc1\n"
        "s_waitcnt vmcnt(9)\n"
        "v_mfma_f32_16x16x32_bf16 v[92:95], a[0:3], v[156:159], 0\n"
        "s_add_u32 m0, s74, 0x1000\n"
        "s_nop 0\n"
        "buffer_load_dwordx4 v29, s[24:27], s77 offen offset:1024 lds\n"
        "v_mfma_f32_16x16x32_bf16 v[96:99], a[4:7], v[156:159], 0\n"
        "buffer_load_dwordx4 v[192:195], v15, s[40:43], s70 offen offset:2048 sc1\n"
        "v_mfma_f32_16x16x32_bf16 v[124:127], a[16:19], v[156:159], 0\n"
        "v_mfma_f32_16x16x32_bf16 v[128:131], a[20:23], v[156:159], 0\n"
        "s_waitcnt vmcnt(10)\n"
        "v_mfma_f32_16x16x32_bf16 v[100:103], a[0:3], v[160:163], 0\n"
        "s_add_u32 s70, s82, s79\n"
        "buffer_load_dwordx4 v[196:199], v12, s[40:43], s70 offen sc1\n"
        "v_mfma_f32_16x16x32_bf16 v[104:107], a[4:7], v[160:163], 0\n"
        "v_mfma_f32_16x16x32_bf16 v[132:135], a[16:19], v[160:163], 0\n"
        "s_waitcnt lgkmcnt(15)\n"
        "s_mul_i32 s77, s84, 0x8\n"
        "v_mfma_f32_16x16x32_bf16 v[136:139], a[20:23], v[160:163], 0\n"
        "s_add_u32 s77, s77, s80\n"
        "s_add_u32 m0, s74, 0x0\n"
        "s_waitcnt vmcnt(10)\n"
        "v_mfma_f32_16x16x32_bf16 v[76:79], a[32:35], v[164:167], v[76:79]\n"
        "s_add_u32 s77, s77, 0x8000\n"
        "buffer_load_dwordx4 v28, s[24:27], s77 offen lds\n"
        "v_mfma_f32_16x16x32_bf16 v[80:83], a[36:39], v[164:167], v[80:83]\n"
        "buffer_load_dwordx4 v[200:203], v12, s[40:43], s70 offen offset:2048 sc1\n"
        "v_mfma_f32_16x16x32_bf16 v[108:111], a[48:51], v[164:167], v[108:111]\n"
        "v_mfma_f32_16x16x32_bf16 v[112:115], a[52:55], v[164:167], v[112:115]\n"
        "s_waitcnt vmcnt(11)\n"
        "v_mfma_f32_16x16x32_bf16 v[84:87], a[32:35], v[168:171], v[84:87]\n"
        "s_add_u32 m0, s74, 0x0\n"
        "s_nop 0\n"
        "buffer_load_dwordx4 v29, s[24:27], s77 offen offset:1024 lds\n"
        "v_mfma_f32_16x16x32_bf16 v[88:91], a[36:39], v[168:171], v[88:91]\n"
        "buffer_load_dwordx4 v[204:207], v15, s[40:43], s70 offen sc1\n"
        "v_mfma_f32_16x16x32_bf16 v[116:119], a[48:51], v[168:171], v[116:119]\n"
        "v_mfma_f32_16x16x32_bf16 v[120:123], a[52:55], v[168:171], v[120:123]\n"
        "s_waitcnt vmcnt(12)\n"
        "v_mfma_f32_16x16x32_bf16 v[92:95], a[32:35], v[172:175], v[92:95]\n"
        "buffer_load_dwordx4 v[208:211], v15, s[40:43], s70 offen offset:2048 sc1\n"
        "v_mfma_f32_16x16x32_bf16 v[96:99], a[36:39], v[172:175], v[96:99]\n"
        "v_mfma_f32_16x16x32_bf16 v[124:127], a[48:51], v[172:175], v[124:127]\n"
        "s_add_u32 m0, s75, 0x1000\n"
        "s_add_u32 s77, s77, 0x1000\n"
        "v_mfma_f32_16x16x32_bf16 v[128:131], a[52:55], v[172:175], v[128:131]\n"
        "buffer_load_dwordx4 v28, s[24:27], s77 offen lds\n"
        "s_waitcnt vmcnt(13)\n"
        "v_mfma_f32_16x16x32_bf16 v[100:103], a[32:35], v[176:179], v[100:103]\n"
        "s_add_u32 s70, s83, s79\n"
        "buffer_load_dwordx4 v[212:215], v12, s[40:43], s70 offen sc1\n"
        "v_mfma_f32_16x16x32_bf16 v[104:107], a[36:39], v[176:179], v[104:107]\n"
        "v_mfma_f32_16x16x32_bf16 v[132:135], a[48:51], v[176:179], v[132:135]\n"
        "v_mfma_f32_16x16x32_bf16 v[136:139], a[52:55], v[176:179], v[136:139]\n"
        "s_waitcnt vmcnt(13)\n"
        "v_mfma_f32_16x16x32_bf16 v[76:79], a[64:67], v[180:183], v[76:79]\n"
        "s_add_u32 m0, s75, 0x1000\n"
        "s_nop 0\n"
        "buffer_load_dwordx4 v29, s[24:27], s77 offen offset:1024 lds\n"
        "v_mfma_f32_16x16x32_bf16 v[80:83], a[68:71], v[180:183], v[80:83]\n"
        "buffer_load_dwordx4 v[216:219], v12, s[40:43], s70 offen offset:2048 sc1\n"
        "v_mfma_f32_16x16x32_bf16 v[108:111], a[80:83], v[180:183], v[108:111]\n"
        "v_mfma_f32_16x16x32_bf16 v[112:115], a[84:87], v[180:183], v[112:115]\n"
        "s_waitcnt vmcnt(13)\n"
        "v_mfma_f32_16x16x32_bf16 v[84:87], a[64:67], v[184:187], v[84:87]\n"
        "buffer_load_dwordx4 v[220:223], v15, s[40:43], s70 offen sc1\n"
        "v_mfma_f32_16x16x32_bf16 v[88:91], a[68:71], v[184:187], v[88:91]\n"
        "v_mfma_f32_16x16x32_bf16 v[116:119], a[80:83], v[184:187], v[116:119]\n"
        "s_mul_i32 s77, s85, 0x8\n"
        "s_add_u32 s77, s77, s80\n"
        "v_mfma_f32_16x16x32_bf16 v[120:123], a[84:87], v[184:187], v[120:123]\n"
        "s_add_u32 m0, s75, 0x1800\n"
        "s_add_u32 s77, s77, 0x8000\n"
        "s_waitcnt vmcnt(13)\n"
        "v_mfma_f32_16x16x32_bf16 v[92:95], a[64:67], v[188:191], v[92:95]\n"
        "buffer_load_dwordx4 v28, s[24:27], s77 offen lds\n"
        "s_add_u32 s28, s69, 0x2800\n"
        "v_mfma_f32_16x16x32_bf16 v[96:99], a[68:71], v[188:191], v[96:99]\n"
        "buffer_load_dwordx2 v[20:21], v11, s[40:43], s28 offen sc1\n"
        "buffer_load_dwordx4 v[224:227], v15, s[40:43], s70 offen offset:2048 sc1\n"
        "v_mfma_f32_16x16x32_bf16 v[124:127], a[80:83], v[188:191], v[124:127]\n"
        "v_mfma_f32_16x16x32_bf16 v[128:131], a[84:87], v[188:191], v[128:131]\n"
        "s_waitcnt vmcnt(14)\n"
        "v_mfma_f32_16x16x32_bf16 v[100:103], a[64:67], v[192:195], v[100:103]\n"
        "ds_read_b64_tr_b16 a[138:139], v30 offset:4096\n"
        "ds_read_b64_tr_b16 a[142:143], v30 offset:4104\n"
        "v_mfma_f32_16x16x32_bf16 v[104:107], a[68:71], v[192:195], v[104:107]\n"
        "ds_read_b64_tr_b16 a[154:155], v31 offset:4096\n"
        "ds_read_b64_tr_b16 a[158:159], v31 offset:4104\n"
        "v_mfma_f32_16x16x32_bf16 v[132:135], a[80:83], v[192:195], v[132:135]\n"
        "s_add_u32 m0, s75, 0x1800\n"
        "s_nop 0\n"
        "buffer_load_dwordx4 v29, s[24:27], s77 offen offset:1024 lds\n"
        "v_mfma_f32_16x16x32_bf16 v[136:139], a[84:87], v[192:195], v[136:139]\n"
        "s_add_u32 s70, s84, s79\n"
        "buffer_load_dwordx4 v[148:151], v12, s[40:43], s70 offen sc1\n"
        "s_waitcnt vmcnt(15)\n"
        "v_mfma_f32_16x16x32_bf16 v[76:79], a[96:99], v[196:199], v[76:79]\n"
        "buffer_load_dwordx4 v[152:155], v12, s[40:43], s70 offen offset:2048 sc1\n"
        "v_mfma_f32_16x16x32_bf16 v[80:83], a[100:103], v[196:199], v[80:83]\n"
        "v_mfma_f32_16x16x32_bf16 v[108:111], a[112:115], v[196:199], v[108:111]\n"
        "s_add_u32 m0, s75, 0x2000\n"
        "s_add_u32 s77, s77, 0x1000\n"
        "v_mfma_f32_16x16x32_bf16 v[112:115], a[116:119], v[196:199], v[112:115]\n"
        "buffer_load_dwordx4 v28, s[24:27], s77 offen lds\n"
        "s_waitcnt vmcnt(15)\n"
        "v_mfma_f32_16x16x32_bf16 v[84:87], a[96:99], v[200:203], v[84:87]\n"
        "buffer_load_dwordx4 v[156:159], v15, s[40:43], s70 offen sc1\n"
        "v_mfma_f32_16x16x32_bf16 v[88:91], a[100:103], v[200:203], v[88:91]\n"
        "v_mfma_f32_16x16x32_bf16 v[116:119], a[112:115], v[200:203], v[116:119]\n"
        "v_mfma_f32_16x16x32_bf16 v[120:123], a[116:119], v[200:203], v[120:123]\n"
        "s_waitcnt vmcnt(14)\n"
        "v_mfma_f32_16x16x32_bf16 v[92:95], a[96:99], v[204:207], v[92:95]\n"
        "ds_read_b64_tr_b16 a[168:169], v30\n"
        "ds_read_b64_tr_b16 a[172:173], v30 offset:8\n"
        "v_mfma_f32_16x16x32_bf16 v[96:99], a[100:103], v[204:207], v[96:99]\n"
        "ds_read_b64_tr_b16 a[184:185], v31\n"
        "ds_read_b64_tr_b16 a[188:189], v31 offset:8\n"
        "v_mfma_f32_16x16x32_bf16 v[124:127], a[112:115], v[204:207], v[124:127]\n"
        "s_add_u32 m0, s75, 0x2000\n"
        "s_nop 0\n"
        "buffer_load_dwordx4 v29, s[24:27], s77 offen offset:1024 lds\n"
        "v_mfma_f32_16x16x32_bf16 v[128:131], a[116:119], v[204:207], v[128:131]\n"
        "buffer_load_dwordx4 v[160:163], v15, s[40:43], s70 offen offset:2048 sc1\n"
        "s_waitcnt vmcnt(15)\n"
        "v_mfma_f32_16x16x32_bf16 v[100:103], a[96:99], v[208:211], v[100:103]\n"
        "s_add_u32 s70, s85, s79\n"
        "buffer_load_dwordx4 v[164:167], v12, s[40:43], s70 offen sc1\n"
        "v_mfma_f32_16x16x32_bf16 v[104:107], a[100:103], v[208:211], v[104:107]\n"
        "v_mfma_f32_16x16x32_bf16 v[132:135], a[112:115], v[208:211], v[132:135]\n"
        "v_mfma_f32_16x16x32_bf16 v[136:139], a[116:119], v[208:211], v[136:139]\n"
        "s_waitcnt vmcnt(14)\n"
        "v_mfma_f32_16x16x32_bf16 v[76:79], a[128:131], v[212:215], v[76:79]\n"
        "buffer_load_dwordx4 v[168:171], v12, s[40:43], s70 offen offset:2048 sc1\n"
        "v_mfma_f32_16x16x32_bf16 v[80:83], a[132:135], v[212:215], v[80:83]\n"
        "v_mfma_f32_16x16x32_bf16 v[108:111], a[144:147], v[212:215], v[108:111]\n"
        "v_mfma_f32_16x16x32_bf16 v[112:115], a[148:151], v[212:215], v[112:115]\n"
        "s_branch .Lku6\n"
        ".Lkt6:\n"
        "s_setpc_b64 s[86:87]\n"
        ".Lku6:\n"
        "s_waitcnt vmcnt(13)\n"
        "v_mfma_f32_16x16x32_bf16 v[84:87], a[128:131], v[216:219], v[84:87]\n"
        "ds_read_b64_tr_b16 a[170:171], v32 offset:4096\n"
        "ds_read_b64_tr_b16 a[174:175], v32 offset:4104\n"
        "v_mfma_f32_16x16x32_bf16 v[88:91], a[132:135], v[216:219], v[88:91]\n"
        "ds_read_b64_tr_b16 a[186:187], v33 offset:4096\n"
        "ds_read_b64_tr_b16 a[190:191], v33 offset:4104\n"
        "v_mfma_f32_16x16x32_bf16 v[116:119], a[144:147], v[216:219], v[116:119]\n"
        "buffer_load_dwordx4 v[172:175], v15, s[40:43], s70 offen sc1\n"
        "v_mfma_f32_16x16x32_bf16 v[120:123], a[148:151], v[216:219], v[120:123]\n"
        "s_waitcnt vmcnt(11)\n"
        "v_cmp_ne_u64 vcc, s[56:57], v[20:21]\n"
        "s_nop 0\n"
        "s_cbranch_vccnz .Lkp6\n"
        ".Lkq6:\n"
        "v_mfma_f32_16x16x32_bf16 v[92:95], a[128:131], v[220:223], v[92:95]\n"
        "buffer_load_dwordx4 v[176:179], v15, s[40:43], s70 offen offset:2048 sc1\n"
        "s_add_u32 s79, s79, 0xe000\n"
        "v_mfma_f32_16x16x32_bf16 v[96:99], a[132:135], v[220:223], v[96:99]\n"
        "v_mfma_f32_16x16x32_bf16 v[124:127], a[144:147], v[220:223], v[124:127]\n"
        "v_mfma_f32_16x16x32_bf16 v[128:131], a[148:151], v[220:223], v[128:131]\n"
        "s_waitcnt vmcnt(11)\n"
        "v_mfma_f32_16x16x32_bf16 v[100:103], a[128:131], v[224:227], v[100:103]\n"
        "s_add_u32 s70, s60, s79\n"
        "buffer_load_dwordx4 v[180:183], v12, s[40:43], s70 offen sc1\n"
        "v_mfma_f32_16x16x32_bf16 v[104:107], a[132:135], v[224:227], v[104:107]\n"
        "v_mfma_f32_16x16x32_bf16 v[132:135], a[144:147], v[224:227], v[132:135]\n"
        "v_mfma_f32_16x16x32_bf16 v[136:139], a[148:151], v[224:227], v[136:139]\n"
        "s_waitcnt vmcnt(10)\n"
        "v_mfma_f32_16x16x32_bf16 v[76:79], a[160:163], v[148:151], v[76:79]\n"
        "ds_read_b64_tr_b16 a[200:201], v32 offset:6144\n"
        "ds_read_b64_tr_b16 a[204:205], v32 offset:6152\n"
        "v_mfma_f32_16x16x32_bf16 v[80:83], a[164:167], v[148:151], v[80:83]\n"
        "ds_read_b64_tr_b16 a[216:217], v33 offset:6144\n"
        "ds_read_b64_tr_b16 a[220:221], v33 offset:6152\n"
        "v_mfma_f32_16x16x32_bf16 v[108:111], a[176:179], v[148:151], v[108:111]\n"
        "buffer_load_dwordx4 v[184:187], v12, s[40:43], s70 offen offset:2048 sc1\n"
        "v_mfma_f32_16x16x32_bf16 v[112:115], a[180:183], v[148:151], v[112:115]\n"
        "s_waitcnt vmcnt(10)\n"
        "v_mfma_f32_16x16x32_bf16 v[84:87], a[160:163], v[152:155], v[84:87]\n"
        "buffer_load_dwordx4 v[188:191], v15, s[40:43], s70 offen sc1\n"
        "v_mfma_f32_16x16x32_bf16 v[88:91], a[164:167], v[152:155], v[88:91]\n"
        "v_mfma_f32_16x16x32_bf16 v[116:119], a[176:179], v[152:155], v[116:119]\n"
        "v_mfma_f32_16x16x32_bf16 v[120:123], a[180:183], v[152:155], v[120:123]\n"
        "s_waitcnt vmcnt(9)\n"
        "v_mfma_f32_16x16x32_bf16 v[92:95], a[160:163], v[156:159], v[92:95]\n"
        "buffer_load_dwordx4 v[192:195], v15, s[40:43], s70 offen offset:2048 sc1\n"
        "v_mfma_f32_16x16x32_bf16 v[96:99], a[164:167], v[156:159], v[96:99]\n"
        "v_mfma_f32_16x16x32_bf16 v[124:127], a[176:179], v[156:159], v[124:127]\n"
        "v_mfma_f32_16x16x32_bf16 v[128:131], a[180:183], v[156:159], v[128:131]\n"
        "s_waitcnt vmcnt(8)\n"
        "v_mfma_f32_16x16x32_bf16 v[100:103], a[160:163], v[160:163], v[100:103]\n"
        "ds_read_b64_tr_b16 a[202:203], v32 offset:8192\n"
        "ds_read_b64_tr_b16 a[206:207], v32 offset:8200\n"
        "v_mfma_f32_16x16x32_bf16 v[104:107], a[164:167], v[160:163], v[104:107]\n"
        "ds_read_b64_tr_b16 a[218:219], v33 offset:8192\n"
        "ds_read_b64_tr_b16 a[222:223], v33 offset:8200\n"
        "v_mfma_f32_16x16x32_bf16 v[132:135], a[176:179], v[160:163], v[132:135]\n"
        "s_add_u32 s70, s68, s79\n"
        "buffer_load_dwordx4 v[196:199], v12, s[40:43], s70 offen sc1\n"
        "v_mfma_f32_16x16x32_bf16 v[136:139], a[180:183], v[160:163], v[136:139]\n"
        "s_waitcnt vmcnt(8)\n"
        "v_mfma_f32_16x16x32_bf16 v[76:79], a[192:195], v[164:167], v[76:79]\n"
        "buffer_load_dwordx4 v[200:203], v12, s[40:43], s70 offen offset:2048 sc1\n"
        "v_mfma_f32_16x16x32_bf16 v[80:83], a[196:199], v[164:167], v[80:83]\n"
        "v_mfma_f32_16x16x32_bf16 v[108:111], a[208:211], v[164:167], v[108:111]\n"
        "v_mfma_f32_16x16x32_bf16 v[112:115], a[212:215], v[164:167], v[112:115]\n"
        "s_waitcnt vmcnt(8)\n"
        "v_mfma_f32_16x16x32_bf16 v[84:87], a[192:195], v[168:171], v[84:87]\n"
        "buffer_load_dwordx4 v[204:207], v15, s[40:43], s70 offen sc1\n"
        "v_mfma_f32_16x16x32_bf16 v[88:91], a[196:199], v[168:171], v[88:91]\n"
        "v_mfma_f32_16x16x32_bf16 v[116:119], a[208:211], v[168:171], v[116:119]\n"
        "v_mfma_f32_16x16x32_bf16 v[120:123], a[212:215], v[168:171], v[120:123]\n"
        "s_waitcnt vmcnt(8)\n"
        "v_mfma_f32_16x16x32_bf16 v[92:95], a[192:195], v[172:175], v[92:95]\n"
        "buffer_load_dwordx4 v[208:211], v15, s[40:43], s70 offen offset:2048 sc1\n"
        "v_mfma_f32_16x16x32_bf16 v[96:99], a[196:199], v[172:175], v[96:99]\n"
        "v_mfma_f32_16x16x32_bf16 v[124:127], a[208:211], v[172:175], v[124:127]\n"
        "v_mfma_f32_16x16x32_bf16 v[128:131], a[212:215], v[172:175], v[128:131]\n"
        "s_waitcnt vmcnt(8)\n"
        "v_mfma_f32_16x16x32_bf16 v[100:103], a[192:195], v[176:179], v[100:103]\n"
        "s_add_u32 s70, s81, s79\n"
        "buffer_load_dwordx4 v[212:215], v12, s[40:43], s70 offen sc1\n"
        "v_mfma_f32_16x16x32_bf16 v[104:107], a[196:199], v[176:179], v[104:107]\n"
        "v_mfma_f32_16x16x32_bf16 v[132:135], a[208:211], v[176:179], v[132:135]\n"
        "v_mfma_f32_16x16x32_bf16 v[136:139], a[212:215], v[176:179], v[136:139]\n"
        "s_waitcnt vmcnt(8)\n"
        "v_mfma_f32_16x16x32_bf16 v[76:79], a[8:11], v[180:183], v[76:79]\n"
        "buffer_load_dwordx4 v[216:219], v12, s[40:43], s70 offen offset:2048 sc1\n"
        "v_mfma_f32_16x16x32_bf16 v[80:83], a[12:15], v[180:183], v[80:83]\n"
        "v_mfma_f32_16x16x32_bf16 v[108:111], a[24:27], v[180:183], v[108:111]\n"
        "v_mfma_f32_16x16x32_bf16 v[112:115], a[28:31], v[180:183], v[112:115]\n"
        "s_waitcnt vmcnt(8)\n"
        "v_mfma_f32_16x16x32_bf16 v[84:87], a[8:11], v[184:187], v[84:87]\n"
        "buffer_load_dwordx4 v[220:223], v15, s[40:43], s70 offen sc1\n"
        "v_mfma_f32_16x16x32_bf16 v[88:91], a[12:15], v[184:187], v[88:91]\n"
        "v_mfma_f32_16x16x32_bf16 v[116:119], a[24:27], v[184:187], v[116:119]\n"
        "v_mfma_f32_16x16x32_bf16 v[120:123], a[28:31], v[184:187], v[120:123]\n"
        "s_waitcnt vmcnt(8)\n"
        "v_mfma_f32_16x16x32_bf16 v[92:95], a[8:11], v[188:191], v[92:95]\n"
        "buffer_load_dwordx4 v[224:227], v15, s[40:43], s70 offen offset:2048 sc1\n"
        "v_mfma_f32_16x16x32_bf16 v[96:99], a[12:15], v[188:191], v[96:99]\n"
        "v_mfma_f32_16x16x32_bf16 v[124:127], a[24:27], v[188:191], v[124:127]\n"
        "v_mfma_f32_16x16x32_bf16 v[128:131], a[28:31], v[188:191], v[128:131]\n"
        "s_waitcnt vmcnt(8)\n"
        "v_mfma_f32_16x16x32_bf16 v[100:103], a[8:11], v[192:195], v[100:103]\n"
        "s_add_u32 s70, s82, s79\n"
        "buffer_load_dwordx4 v[148:151], v12, s[40:43], s70 offen sc1\n"
        "v_mfma_f32_16x16x32_bf16 v[104:107], a[12:15], v[192:195], v[104:107]\n"
        "v_mfma_f32_16x16x32_bf16 v[132:135], a[24:27], v[192:195], v[132:135]\n"
        "v_mfma_f32_16x16x32_bf16 v[136:139], a[28:31], v[192:195], v[136:139]\n"
        "s_waitcnt vmcnt(8)\n"
        "v_mfma_f32_16x16x32_bf16 v[76:79], a[40:43], v[196:199], v[76:79]\n"
        "buffer_load_dwordx4 v[152:155], v12, s[40:43], s70 offen offset:2048 sc1\n"
        "v_mfma_f32_16x16x32_bf16 v[80:83], a[44:47], v[196:199], v[80:83]\n"
        "v_mfma_f32_16x16x32_bf16 v[108:111], a[56:59], v[196:199], v[108:111]\n"
        "v_mfma_f32_16x16x32_bf16 v[112:115], a[60:63], v[196:199], v[112:115]\n"
        "s_waitcnt vmcnt(8)\n"
        "v_mfma_f32_16x16x32_bf16 v[84:87], a[40:43], v[200:203], v[84:87]\n"
        "buffer_load_dwordx4 v[156:159], v15, s[40:43], s70 offen sc1\n"
        "v_mfma_f32_16x16x32_bf16 v[88:91], a[44:47], v[200:203], v[88:91]\n"
        "v_mfma_f32_16x16x32_bf16 v[116:119], a[56:59], v[200:203], v[116:119]\n"
        "v_mfma_f32_16x16x32_bf16 v[120:123], a[60:63], v[200:203], v[120:123]\n"
        "s_waitcnt vmcnt(8)\n"
        "v_mfma_f32_16x16x32_bf16 v[92:95], a[40:43], v[204:207], v[92:95]\n"
        "buffer_load_dwordx4 v[160:163], v15, s[40:43], s70 offen offset:2048 sc1\n"
        "v_mfma_f32_16x16x32_bf16 v[96:99], a[44:47], v[204:207], v[96:99]\n"
        "v_mfma_f32_16x16x32_bf16 v[124:127], a[56:59], v[204:207], v[124:127]\n"
        "v_mfma_f32_16x16x32_bf16 v[128:131], a[60:63], v[204:207], v[128:131]\n"
        "s_waitcnt vmcnt(8)\n"
        "v_mfma_f32_16x16x32_bf16 v[100:103], a[40:43], v[208:211], v[100:103]\n"
        "s_add_u32 s70, s83, s79\n"
        "buffer_load_dwordx4 v[164:167], v12, s[40:43], s70 offen sc1\n"
        "v_mfma_f32_16x16x32_bf16 v[104:107], a[44:47], v[208:211], v[104:107]\n"
        "v_mfma_f32_16x16x32_bf16 v[132:135], a[56:59], v[208:211], v[132:135]\n"
        "v_mfma_f32_16x16x32_bf16 v[136:139], a[60:63], v[208:211], v[136:139]\n"
        "s_waitcnt vmcnt(8) lgkmcnt(15)\n"
        "v_mfma_f32_16x16x32_bf16 v[76:79], a[72:75], v[212:215], v[76:79]\n"
        "buffer_load_dwordx4 v[168:171], v12, s[40:43], s70 offen offset:2048 sc1\n"
        "v_mfma_f32_16x16x32_bf16 v[80:83], a[76:79], v[212:215], v[80:83]\n"
        "v_mfma_f32_16x16x32_bf16 v[108:111], a[88:91], v[212:215], v[108:111]\n"
        "v_mfma_f32_16x16x32_bf16 v[112:115], a[92:95], v[212:215], v[112:115]\n"
        "s_waitcnt vmcnt(8)\n"
        "v_mfma_f32_16x16x32_bf16 v[84:87], a[72:75], v[216:219], v[84:87]\n"
        "buffer_load_dwordx4 v[172:175], v15, s[40:43], s70 offen sc1\n"
        "v_mfma_f32_16x16x32_bf16 v[88:91], a[76:79], v[216:219], v[88:91]\n"
        "v_mfma_f32_16x16x32_bf16 v[116:119], a[88:91], v[216:219], v[116:119]\n"
        "v_mfma_f32_16x16x32_bf16 v[120:123], a[92:95], v[216:219], v[120:123]\n"
        "s_waitcnt vmcnt(8)\n"
        "v_mfma_f32_16x16x32_bf16 v[92:95], a[72:75], v[220:223], v[92:95]\n"
        "buffer_load_dwordx4 v[176:179], v15, s[40:43], s70 offen offset:2048 sc1\n"
        "ds_read_b128 v[60:63], v10 offset:256\n"
        "v_mfma_f32_16x16x32_bf16 v[96:99], a[76:79], v[220:223], v[96:99]\n"
        "ds_read_b128 v[64:67], v10 offset:272\n"
        "s_xor_b32 s55, s50, 1\n"
        "v_mfma_f32_16x16x32_bf16 v[124:127], a[88:91], v[220:223], v[124:127]\n"
        "s_mul_i32 s55, s55, 0x3000\n"
        "v_add_u32 v6, s55, v2\n"
        "v_mfma_f32_16x16x32_bf16 v[128:131], a[92:95], v[220:223], v[128:131]\n"
        "s_xor_b32 s55, s50, 2\n"
        "s_mul_i32 s55, s55, 0x3000\n"
        "s_waitcnt vmcnt(8)\n"
        "v_mfma_f32_16x16x32_bf16 v[100:103], a[72:75], v[224:227], v[100:103]\n"
        "v_add_u32 v7, s55, v2\n"
        "s_xor_b32 s55, s50, 3\n"
        "v_mfma_f32_16x16x32_bf16 v[104:107], a[76:79], v[224:227], v[104:107]\n"
        "s_mul_i32 s55, s55, 0x3000\n"
        "v_add_u32 v8, s55, v2\n"
        "v_mfma_f32_16x16x32_bf16 v[132:135], a[88:91], v[224:227], v[132:135]\n"
        "s_mul_i32 s55, s50, 0x3000\n"
        "v_add_u32 v11, s55, v2\n"
        "v_mfma_f32_16x16x32_bf16 v[136:139], a[92:95], v[224:227], v[136:139]\n"
        "s_waitcnt lgkmcnt(0)\n"
        "v_mul_f32 v44, v60, v16\n"
        "s_waitcnt vmcnt(7)\n"
        "v_mfma_f32_16x16x32_bf16 v[76:79], a[104:107], v[148:151], v[76:79]\n"
        "v_mul_f32 v45, v61, v16\n"
        "v_mul_f32 v46, v62, v16\n"
        "v_mfma_f32_16x16x32_bf16 v[80:83], a[108:111], v[148:151], v[80:83]\n"
        "v_mul_f32 v47, v63, v16\n"
        "v_mul_f32 v48, v64, v16\n"
        "v_mfma_f32_16x16x32_bf16 v[108:111], a[120:123], v[148:151], v[108:111]\n"
        "v_mul_f32 v49, v65, v16\n"
        "v_mul_f32 v50, v66, v16\n"
        "v_mfma_f32_16x16x32_bf16 v[112:115], a[124:127], v[148:151], v[112:115]\n"
        "v_mul_f32 v51, v67, v16\n"
        "v_mul_f32 v52, v60, v17\n"
        "s_waitcnt vmcnt(6)\n"
        "v_mfma_f32_16x16x32_bf16 v[84:87], a[104:107], v[152:155], v[84:87]\n"
        "v_mul_f32 v53, v61, v17\n"
        "v_mul_f32 v54, v62, v17\n"
        "v_mfma_f32_16x16x32_bf16 v[88:91], a[108:111], v[152:155], v[88:91]\n"
        "v_mul_f32 v55, v63, v17\n"
        "v_mul_f32 v56, v64, v17\n"
        "v_mfma_f32_16x16x32_bf16 v[116:119], a[120:123], v[152:155], v[116:119]\n"
        "v_mul_f32 v57, v65, v17\n"
        "v_mul_f32 v58, v66, v17\n"
        "v_mfma_f32_16x16x32_bf16 v[120:123], a[124:127], v[152:155], v[120:123]\n"
        "v_mul_f32 v59, v67, v17\n"
        "s_add_u32 s70, s84, s79\n"
        "s_waitcnt vmcnt(5)\n"
        "v_mfma_f32_16x16x32_bf16 v[92:95], a[104:107], v[156:159], v[92:95]\n"
        "buffer_load_dwordx4 v[180:183], v12, s[40:43], s70 offen sc1\n"
        "buffer_load_dwordx4 v[184:187], v12, s[40:43], s70 offen offset:2048 sc1\n"
        "v_mfma_f32_16x16x32_bf16 v[96:99], a[108:111], v[156:159], v[96:99]\n"
        "buffer_load_dwordx4 v[188:191], v15, s[40:43], s70 offen sc1\n"
        "buffer_load_dwordx4 v[192:195], v15, s[40:43], s70 offen offset:2048 sc1\n"
        "v_mfma_f32_16x16x32_bf16 v[124:127], a[120:123], v[156:159], v[124:127]\n"
        "v_mfma_f32_16x16x32_bf16 v[128:131], a[124:127], v[156:159], v[128:131]\n"
        "s_waitcnt vmcnt(8)\n"
        "v_mfma_f32_16x16x32_bf16 v[100:103], a[104:107], v[160:163], v[100:103]\n"
        "s_add_u32 s70, s85, s79\n"
        "buffer_load_dwordx4 v[196:199], v12, s[40:43], s70 offen sc1\n"
        "v_mfma_f32_16x16x32_bf16 v[104:107], a[108:111], v[160:163], v[104:107]\n"
        "v_mfma_f32_16x16x32_bf16 v[132:135], a[120:123], v[160:163], v[132:135]\n"
        "v_mfma_f32_16x16x32_bf16 v[136:139], a[124:127], v[160:163], v[136:139]\n"
        "s_waitcnt vmcnt(8)\n"
        "v_mfma_f32_16x16x32_bf16 v[76:79], a[136:139], v[164:167], v[76:79]\n"
        "buffer_load_dwordx4 v[200:203], v12, s[40:43], s70 offen offset:2048 sc1\n"
        "v_mfma_f32_16x16x32_bf16 v[80:83], a[140:143], v[164:167], v[80:83]\n"
        "v_mfma_f32_16x16x32_bf16 v[108:111], a[152:155], v[164:167], v[108:111]\n"
        "v_mfma_f32_16x16x32_bf16 v[112:115], a[156:159], v[164:167], v[112:115]\n"
        "s_waitcnt vmcnt(8)\n"
        "v_mfma_f32_16x16x32_bf16 v[84:87], a[136:139], v[168:171], v[84:87]\n"
        "buffer_load_dwordx4 v[204:207], v15, s[40:43], s70 offen sc1\n"
        "v_mfma_f32_16x16x32_bf16 v[88:91], a[140:143], v[168:171], v[88:91]\n"
        "v_mfma_f32_16x16x32_bf16 v[116:119], a[152:155], v[168:171], v[116:119]\n"
        "v_mfma_f32_16x16x32_bf16 v[120:123], a[156:159], v[168:171], v[120:123]\n"
        "s_waitcnt vmcnt(8)\n"
        "v_mfma_f32_16x16x32_bf16 v[92:95], a[136:139], v[172:175], v[92:95]\n"
        "buffer_load_dwordx4 v[208:211], v15, s[40:43], s70 offen offset:2048 sc1\n"
        "v_mfma_f32_16x16x32_bf16 v[96:99], a[140:143], v[172:175], v[96:99]\n"
        "v_mfma_f32_16x16x32_bf16 v[124:127], a[152:155], v[172:175], v[124:127]\n"
        "v_mfma_f32_16x16x32_bf16 v[128:131], a[156:159], v[172:175], v[128:131]\n"
        "s_waitcnt vmcnt(8)\n"
        "v_mfma_f32_16x16x32_bf16 v[100:103], a[136:139], v[176:179], v[100:103]\n"
        "v_mfma_f32_16x16x32_bf16 v[104:107], a[140:143], v[176:179], v[104:107]\n"
        "v_mfma_f32_16x16x32_bf16 v[132:135], a[152:155], v[176:179], v[132:135]\n"
        "v_mfma_f32_16x16x32_bf16 v[136:139], a[156:159], v[176:179], v[136:139]\n"
        "s_waitcnt vmcnt(5)\n"
        "v_mfma_f32_16x16x32_bf16 v[124:127], a[184:187], v[188:191], v[124:127]\n"
        "v_mfma_f32_16x16x32_bf16 v[128:131], a[188:191], v[188:191], v[128:131]\n"
        "s_waitcnt vmcnt(4)\n"
        "v_mfma_f32_16x16x32_bf16 v[132:135], a[184:187], v[192:195], v[132:135]\n"
        "v_mfma_f32_16x16x32_bf16 v[136:139], a[188:191], v[192:195], v[136:139]\n"
        "s_waitcnt vmcnt(1)\n"
        "v_mfma_f32_16x16x32_bf16 v[124:127], a[216:219], v[204:207], v[124:127]\n"
        "v_mfma_f32_16x16x32_bf16 v[128:131], a[220:223], v[204:207], v[128:131]\n"
        "s_waitcnt vmcnt(0)\n"
        "v_mfma_f32_16x16x32_bf16 v[132:135], a[216:219], v[208:211], v[132:135]\n"
        "v_mfma_f32_16x16x32_bf16 v[136:139], a[220:223], v[208:211], v[136:139]\n"
        "v_mfma_f32_16x16x32_bf16 v[108:111], a[184:187], v[180:183], v[108:111]\n"
        "v_mfma_f32_16x16x32_bf16 v[112:115], a[188:191], v[180:183], v[112:115]\n"
        "v_mfma_f32_16x16x32_bf16 v[116:119], a[184:187], v[184:187], v[116:119]\n"
        "v_mfma_f32_16x16x32_bf16 v[120:123], a[188:191], v[184:187], v[120:123]\n"
        "ds_write_b128 v8, v[124:127] offset:8192\n"
        "ds_write_b128 v8, v[128:131] offset:9216\n"
        "v_mfma_f32_16x16x32_bf16 v[108:111], a[216:219], v[196:199], v[108:111]\n"
        "ds_write_b128 v8, v[132:135] offset:10240\n"
        "ds_write_b128 v8, v[136:139] offset:11264\n"
        "v_mfma_f32_16x16x32_bf16 v[112:115], a[220:223], v[196:199], v[112:115]\n"
        "v_mfma_f32_16x16x32_bf16 v[116:119], a[216:219], v[200:203], v[116:119]\n"
        "v_mfma_f32_16x16x32_bf16 v[120:123], a[220:223], v[200:203], v[120:123]\n"
        "v_mfma_f32_16x16x32_bf16 v[92:95], a[168:171], v[188:191], v[92:95]\n"
        "v_mfma_f32_16x16x32_bf16 v[96:99], a[172:175], v[188:191], v[96:99]\n"
        "v_mfma_f32_16x16x32_bf16 v[100:103], a[168:171], v[192:195], v[100:103]\n"
        "v_mfma_f32_16x16x32_bf16 v[104:107], a[172:175], v[192:195], v[104:107]\n"
        "ds_write_b128 v7, v[108:111] offset:4096\n"
        "s_nop 0\n"
        "ds_write_b128 v7, v[112:115] offset:5120\n"
        "v_mfma_f32_16x16x32_bf16 v[92:95], a[200:203], v[204:207], v[92:95]\n"
        "ds_write_b128 v7, v[116:119] offset:6144\n"
        "ds_write_b128 v7, v[120:123] offset:7168\n"
        "v_mfma_f32_16x16x32_bf16 v[96:99], a[204:207], v[204:207], v[96:99]\n"
        "v_mfma_f32_16x16x32_bf16 v[100:103], a[200:203], v[208:211], v[100:103]\n"
        "v_mfma_f32_16x16x32_bf16 v[104:107], a[204:207], v[208:211], v[104:107]\n"
        "v_mfma_f32_16x16x32_bf16 v[76:79], a[168:171], v[180:183], v[76:79]\n"
        "v_mfma_f32_16x16x32_bf16 v[80:83], a[172:175], v[180:183], v[80:83]\n"
        "v_mfma_f32_16x16x32_bf16 v[84:87], a[168:171], v[184:187], v[84:87]\n"
        "v_mfma_f32_16x16x32_bf16 v[88:91], a[172:175], v[184:187], v[88:91]\n"
        "ds_write_b128 v6, v[92:95]\n"
        "s_nop 0\n"
        "ds_write_b128 v6, v[96:99] offset:1024\n"
        "v_mfma_f32_16x16x32_bf16 v[76:79], a[200:203], v[196:199], v[76:79]\n"
        "ds_write_b128 v6, v[100:103] offset:2048\n"
        "ds_write_b128 v6, v[104:107] offset:3072\n"
        "v_mfma_f32_16x16x32_bf16 v[80:83], a[204:207], v[196:199], v[80:83]\n"
        "v_mfma_f32_16x16x32_bf16 v[84:87], a[200:203], v[200:203], v[84:87]\n"
        "v_mfma_f32_16x16x32_bf16 v[88:91], a[204:207], v[200:203], v[88:91]\n"
        "s_waitcnt lgkmcnt(0)\n"
        "s_barrier\n"
        "ds_read_b128 v[148:151], v11\n"
        "ds_read_b128 v[164:167], v11 offset:4096\n"
        "ds_read_b128 v[180:183], v11 offset:8192\n"
        "ds_read_b128 v[152:155], v11 offset:1024\n"
        "ds_read_b128 v[168:171], v11 offset:5120\n"
        "ds_read_b128 v[184:187], v11 offset:9216\n"
        "ds_read_b128 v[156:159], v11 offset:2048\n"
        "ds_read_b128 v[172:175], v11 offset:6144\n"
        "ds_read_b128 v[188:191], v11 offset:10240\n"
        "ds_read_b128 v[160:163], v11 offset:3072\n"
        "ds_read_b128 v[176:179], v11 offset:7168\n"
        "ds_read_b128 v[192:195], v11 offset:11264\n"
        "s_waitcnt lgkmcnt(9)\n"
        "v_pk_add_f32 v[76:77], v[76:77], v[148:149]\n"
        "v_pk_add_f32 v[164:165], v[164:165], v[180:181]\n"
        "v_pk_add_f32 v[76:77], v[76:77], v[164:165]\n"
        "v_pk_fma_f32 v[76:77], v[76:77], v[44:45], v[228:229]\n"
        "v_cvt_pk_bf16_f32 v244, v76, v77\n"
        "v_pk_add_f32 v[78:79], v[78:79], v[150:151]\n"
        "v_pk_add_f32 v[166:167], v[166:167], v[182:183]\n"
        "v_pk_add_f32 v[78:79], v[78:79], v[166:167]\n"
        "v_pk_fma_f32 v[78:79], v[78:79], v[46:47], v[230:231]\n"
        "v_cvt_pk_bf16_f32 v245, v78, v79\n"
        "s_waitcnt lgkmcnt(6)\n"
        "v_pk_add_f32 v[80:81], v[80:81], v[152:153]\n"
        "v_pk_add_f32 v[168:169], v[168:169], v[184:185]\n"
        "v_pk_add_f32 v[80:81], v[80:81], v[168:169]\n"
        "v_pk_fma_f32 v[80:81], v[80:81], v[48:49], v[232:233]\n"
        "v_cvt_pk_bf16_f32 v246, v80, v81\n"
        "v_pk_add_f32 v[82:83], v[82:83], v[154:155]\n"
        "v_pk_add_f32 v[170:171], v[170:171], v[186:187]\n"
        "v_pk_add_f32 v[82:83], v[82:83], v[170:171]\n"
        "v_pk_fma_f32 v[82:83], v[82:83], v[50:51], v[234:235]\n"
        "v_cvt_pk_bf16_f32 v247, v82, v83\n"
        "buffer_store_dwordx4 v[244:247], v9, s[36:39], s31 offen\n"
        "s_waitcnt lgkmcnt(3)\n"
        "v_pk_add_f32 v[84:85], v[84:85], v[156:157]\n"
        "v_pk_add_f32 v[172:173], v[172:173], v[188:189]\n"
        "v_pk_add_f32 v[84:85], v[84:85], v[172:173]\n"
        "v_pk_fma_f32 v[84:85], v[84:85], v[52:53], v[236:237]\n"
        "v_cvt_pk_bf16_f32 v248, v84, v85\n"
        "v_pk_add_f32 v[86:87], v[86:87], v[158:159]\n"
        "v_pk_add_f32 v[174:175], v[174:175], v[190:191]\n"
        "v_pk_add_f32 v[86:87], v[86:87], v[174:175]\n"
        "v_pk_fma_f32 v[86:87], v[86:87], v[54:55], v[238:239]\n"
        "v_cvt_pk_bf16_f32 v249, v86, v87\n"
        "s_waitcnt lgkmcnt(0)\n"
        "v_pk_add_f32 v[88:89], v[88:89], v[160:161]\n"
        "v_pk_add_f32 v[176:177], v[176:177], v[192:193]\n"
        "v_pk_add_f32 v[88:89], v[88:89], v[176:177]\n"
        "v_pk_fma_f32 v[88:89], v[88:89], v[56:57], v[240:241]\n"
        "v_cvt_pk_bf16_f32 v250, v88, v89\n"
        "v_pk_add_f32 v[90:91], v[90:91], v[162:163]\n"
        "v_pk_add_f32 v[178:179], v[178:179], v[194:195]\n"
        "v_pk_add_f32 v[90:91], v[90:91], v[178:179]\n"
        "v_pk_fma_f32 v[90:91], v[90:91], v[58:59], v[242:243]\n"
        "v_cvt_pk_bf16_f32 v251, v90, v91\n"
        "buffer_store_dwordx4 v[248:251], v9, s[36:39], s33 offen\n"
        "s_endpgm\n"
        ".Lkt7:\n"
        "s_setpc_b64 s[86:87]\n"
        ".Lkp0:\n"
        ".Lkp50:\n"
        "s_sleep 1\n"
        "buffer_load_dwordx2 v[20:21], v11, s[40:43], s28 offen sc1\n"
        "s_sub_u32 s54, s54, 1\n"
        "s_cselect_b32 s54, 0, s54\n"
        "s_waitcnt vmcnt(0)\n"
        "s_cbranch_scc1 .Lkq0\n"
        "v_cmp_ne_u64 vcc, s[56:57], v[20:21]\n"
        "s_nop 0\n"
        "s_cbranch_vccnz .Lkp50\n"
        "s_branch .Lkq0\n"
        ".Lkp1:\n"
        ".Lkp51:\n"
        "s_sleep 1\n"
        "buffer_load_dwordx2 v[20:21], v11, s[40:43], s28 offen sc1\n"
        "s_sub_u32 s54, s54, 1\n"
        "s_cselect_b32 s54, 0, s54\n"
        "s_waitcnt vmcnt(0)\n"
        "s_cbranch_scc1 .Lkq1\n"
        "v_cmp_ne_u64 vcc, s[56:57], v[20:21]\n"
        "s_nop 0\n"
        "s_cbranch_vccnz .Lkp51\n"
        "s_branch .Lkq1\n"
        ".Lkp2:\n"
        ".Lkp52:\n"
        "s_sleep 1\n"
        "buffer_load_dwordx2 v[20:21], v15, s[40:43], s28 offen sc1\n"
        "s_sub_u32 s54, s54, 1\n"
        "s_cselect_b32 s54, 0, s54\n"
        "s_waitcnt vmcnt(0)\n"
        "s_cbranch_scc1 .Lkq2\n"
        "v_cmp_ne_u64 vcc, s[56:57], v[20:21]\n"
        "s_nop 0\n"
        "s_cbranch_vccnz .Lkp52\n"
        "s_branch .Lkq2\n"
        ".Lkp3:\n"
        ".Lkp53:\n"
        "s_sleep 1\n"
        "buffer_load_dwordx2 v[20:21], v11, s[40:43], s28 offen sc1\n"
        "s_sub_u32 s54, s54, 1\n"
        "s_cselect_b32 s54, 0, s54\n"
        "s_waitcnt vmcnt(0)\n"
        "s_cbranch_scc1 .Lkq3\n"
        "v_cmp_ne_u64 vcc, s[56:57], v[20:21]\n"
        "s_nop 0\n"
        "s_cbranch_vccnz .Lkp53\n"
        "s_branch .Lkq3\n"
        ".Lkp4:\n"
        ".Lkp54:\n"
        "s_sleep 1\n"
        "buffer_load_dwordx2 v[20:21], v11, s[40:43], s28 offen sc1\n"
        "s_sub_u32 s54, s54, 1\n"
        "s_cselect_b32 s54, 0, s54\n"
        "s_waitcnt vmcnt(0)\n"
        "s_cbranch_scc1 .Lkq4\n"
        "v_cmp_ne_u64 vcc, s[56:57], v[20:21]\n"
        "s_nop 0\n"
        "s_cbranch_vccnz .Lkp54\n"
        "s_branch .Lkq4\n"
        ".Lkp5:\n"
        "s_waitcnt vmcnt(0)\n"
        "s_add_u32 s59, s64, 0x2800\n"
        "buffer_store_dwordx4 v[252:255], v19, s[40:43], s59 offen sc1\n"
        ".Lkp55:\n"
        "s_sleep 1\n"
        "buffer_load_dwordx2 v[20:21], v11, s[40:43], s28 offen sc1\n"
        "s_sub_u32 s54, s54, 1\n"
        "s_cselect_b32 s54, 0, s54\n"
        "s_waitcnt vmcnt(0)\n"
        "s_cbranch_scc1 .Lkq5\n"
        "v_cmp_ne_u64 vcc, s[56:57], v[20:21]\n"
        "s_nop 0\n"
        "s_cbranch_vccnz .Lkp55\n"
        "s_branch .Lkq5\n"
        ".Lkp6:\n"
        ".Lkp56:\n"
        "s_sleep 1\n"
        "buffer_load_dwordx2 v[20:21], v11, s[40:43], s28 offen sc1\n"
        "s_sub_u32 s54, s54, 1\n"
        "s_cselect_b32 s54, 0, s54\n"
        "s_waitcnt vmcnt(0)\n"
        "s_cbranch_scc1 .Lkq6\n"
        "v_cmp_ne_u64 vcc, s[56:57], v[20:21]\n"
        "s_nop 0\n"
        "s_cbranch_vccnz .Lkp56\n"
        "s_branch .Lkq6\n"
        ".Lkend:\n"
        :
        : "{s[0:1]}"(__builtin_amdgcn_kernarg_segment_ptr()), "{s2}"(__builtin_amdgcn_workgroup_id_x()),
          "{v0}"(__builtin_amdgcn_workitem_id_x()), "{s3}"((unsigned)(size_t)(__attribute__((address_space(3))) char*)smem)
        : "memory", "vcc", "scc", "v1", "v2", "v3", "v4", "v5", "v6", "v7", "v8", "v9", "v10", "v11", "v12", "v13", "v14", "v15", "v16",
          "v17", "v18", "v19", "v20", "v21", "v22", "v23", "v24", "v25", "v26", "v27", "v28", "v29", "v30", "v31", "v32",
          "v33", "v34", "v35", "v36", "v37", "v38", "v39", "v40", "v41", "v42", "v43", "v44", "v45", "v46", "v47", "v48",
          "v49", "v50", "v51", "v52", "v53", "v54", "v55", "v56", "v57", "v58", "v59", "v60", "v61", "v62", "v63", "v64",
          "v65", "v66", "v67", "v68", "v69", "v70", "v71", "v72", "v73", "v74", "v75", "v76", "v77", "v78", "v79", "v80",
          "v81", "v82", "v83", "v84", "v85", "v86", "v87", "v88", "v89", "v90", "v91", "v92", "v93", "v94", "v95", "v96",
          "v97", "v98", "v99", "v100", "v101", "v102", "v103", "v104", "v105", "v106", "v107", "v108", "v109", "v110", "v111", "v112",
          "v113", "v114", "v115", "v116", "v117", "v118", "v119", "v120", "v121", "v122", "v123", "v124", "v125", "v126", "v127", "v128",
          "v129", "v130", "v131", "v132", "v133", "v134", "v135", "v136", "v137", "v138", "v139", "v140", "v141", "v142", "v143", "v144",
          "v145", "v146", "v147", "v148", "v149", "v150", "v151", "v152", "v153", "v154", "v155", "v156", "v157", "v158", "v159", "v160",
          "v161", "v162", "v163", "v164", "v165", "v166", "v167", "v168", "v169", "v170", "v171", "v172", "v173", "v174", "v175", "v176",
          "v177", "v178", "v179", "v180", "v181", "v182", "v183", "v184", "v185", "v186", "v187", "v188", "v189", "v190", "v191", "v192",
          "v193", "v194", "v195", "v196", "v197", "v198", "v199", "v200", "v201", "v202", "v203", "v204", "v205", "v206", "v207", "v208",
          "v209", "v210", "v211", "v212", "v213", "v214", "v215", "v216", "v217", "v218", "v219", "v220", "v221", "v222", "v223", "v224",
          "v225", "v226", "v227", "v228", "v229", "v230", "v231", "v232", "v233", "v234", "v235", "v236", "v237", "v238", "v239", "v240",
          "v241", "v242", "v243", "v244", "v245", "v246", "v247", "v248", "v249", "v250", "v251", "v252", "v253", "v254", "v255", "a0",
          "a1", "a2", "a3", "a4", "a5", "a6", "a7", "a8", "a9", "a10", "a11", "a12", "a13", "a14", "a15", "a16",
          "a17", "a18", "a19", "a20", "a21", "a22", "a23", "a24", "a25", "a26", "a27", "a28", "a29", "a30", "a31", "a32",
          "a33", "a34", "a35", "a36", "a37", "a38", "a39", "a40", "a41", "a42", "a43", "a44", "a45", "a46", "a47", "a48",
          "a49", "a50", "a51", "a52", "a53", "a54", "a55", "a56", "a57", "a58", "a59", "a60", "a61", "a62", "a63", "a64",
          "a65", "a66", "a67", "a68", "a69", "a70", "a71", "a72", "a73", "a74", "a75", "a76", "a77", "a78", "a79", "a80",
          "a81", "a82", "a83", "a84", "a85", "a86", "a87", "a88", "a89", "a90", "a91", "a92", "a93", "a94", "a95", "a96",
          "a97", "a98", "a99", "a100", "a101", "a102", "a103", "a104", "a105", "a106", "a107", "a108", "a109", "a110", "a111", "a112",
          "a113", "a114", "a115", "a116", "a117", "a118", "a119", "a120", "a121", "a122", "a123", "a124", "a125", "a126", "a127", "a128",
          "a129", "a130", "a131", "a132", "a133", "a134", "a135", "a136", "a137", "a138", "a139", "a140", "a141", "a142", "a143", "a144",
          "a145", "a146", "a147", "a148", "a149", "a150", "a151", "a152", "a153", "a154", "a155", "a156", "a157", "a158", "a159", "a160",
          "a161", "a162", "a163", "a164", "a165", "a166", "a167", "a168", "a169", "a170", "a171", "a172", "a173", "a174", "a175", "a176",
          "a177", "a178", "a179", "a180", "a181", "a182", "a183", "a184", "a185", "a186", "a187", "a188", "a189", "a190", "a191", "a192",
          "a193", "a194", "a195", "a196", "a197", "a198", "a199", "a200", "a201", "a202", "a203", "a204", "a205", "a206", "a207", "a208",
          "a209", "a210", "a211", "a212", "a213", "a214", "a215", "a216", "a217", "a218", "a219", "a220", "a221", "a222", "a223", "a224",
          "a225", "a226", "a227", "a228", "a229", "a230", "a231", "a232", "a233", "a234", "a235", "a236", "a237", "a238", "a239", "a240",
          "a241", "a242", "a243", "a244", "a245", "a246", "a247", "a248", "a249", "a250", "a251", "a252", "a253", "a254", "a255", "s4",
          "s5", "s6", "s7", "s8", "s9", "s10", "s11", "s12", "s13", "s14", "s15", "s16", "s17", "s18", "s19", "s20",
          "s21", "s22", "s23", "s24", "s25", "s26", "s27", "s28", "s29", "s30", "s31", "s33", "s34", "s35", "s36", "s37",
          "s38", "s39", "s40", "s41", "s42", "s43", "s44", "s45", "s46", "s47", "s48", "s49", "s50", "s51", "s52", "s53",
          "s54", "s55", "s56", "s57", "s58", "s59", "s60", "s61", "s62", "s63", "s64", "s65", "s66", "s67", "s68", "s69",
          "s70", "s71", "s72", "s73", "s74", "s75", "s76", "s77", "s78", "s79", "s80", "s81", "s82", "s83", "s84", "s85",
          "s86", "s87", "s88", "s89", "s90", "s91", "s92", "s93", "s94", "s95", "s96", "s97", "s98", "s99");
#endif
}

extern "C" void kernel_launch(void* const* d_in, const int* in_sizes, int n_in, void* d_out, int out_size, void* d_ws,
                              size_t ws_size, hipStream_t stream) {
    adaln_fnet_ffn_kernel<<<256, 256, 0, stream>>>(d_in[0], d_in[1], d_in[2], d_in[3], d_in[4], d_in[6], d_in[7], d_in[8],
                                                   d_in[9], d_out, d_ws);
}
```
